```python
import torch

def _softmax(x, axis=-1):
    return torch.softmax(x, dim=axis)
def _gelu(x):
    return torch.nn.functional.gelu(x, approximate="tanh")
def _silu(x):
    return torch.nn.functional.silu(x)
def _sigmoid(x):
    return torch.sigmoid(x)
def _layernorm(x, axis=-1, eps=1e-5):
    m = x.mean(dim=axis, keepdim=True)
    v = x.var(dim=axis, keepdim=True, unbiased=False)
    return (x - m) / torch.sqrt(v + eps)
def _scatter_add(tgt, idx, src, axis=0):
    return tgt.clone().index_add_(axis, idx, src)
def _segment_sum(src, seg, n):
    out = torch.zeros((n,) + src.shape[1:], dtype=src.dtype)
    return out.index_add_(0, seg, src)
def _pad(x, pw):
    flat = tuple(v for pair in reversed(pw) for v in pair)
    return torch.nn.functional.pad(x, flat)

kernel_name = 'parallel_sum__ssm_conv_h3__moe_arch_mod__prompt'

def setup_inputs():

    _w = (lambda a: (lambda t: t.to(torch.bfloat16) if t.is_floating_point() else t)(torch.as_tensor(a)))
    def _A_setup():
        B, S, D = 1, 2048, 640
        ks = 4
        return {
            "q": _w((lambda *s: torch.randn(*s, dtype=torch.bfloat16) * 0.05)(B, S, D)),
            "k": _w((lambda *s: torch.randn(*s, dtype=torch.bfloat16) * 0.4)(B, S, D)),
            "v": _w((lambda *s: torch.randn(*s, dtype=torch.bfloat16) * 0.4)(B, S, D)),
            "shift_k": _w((lambda *s: torch.randn(*s, dtype=torch.bfloat16) * 0.4)(D, ks) * 0.5),
            "ssm_k": _w((lambda *s: torch.randn(*s, dtype=torch.bfloat16) * 1.6)(D, S) * (1.0 / S ** 0.5)),
            "ssm_D": _w((lambda *s: torch.randn(*s, dtype=torch.bfloat16) * 0.4)(D)),
        }
    _A_in = _A_setup()

    def _B_setup():
        B, S, D, F, C = 1, 2048, 640, 1728, 2
        return {
            "x": _w((lambda *s: torch.randn(*s, dtype=torch.bfloat16) * 0.05)(B, S, D)),
            "w_r": _w((lambda *s: torch.randn(*s, dtype=torch.bfloat16) * 0.05)(D)),
            "w_gate": _w((lambda *s: torch.randn(*s, dtype=torch.bfloat16) * 0.05)(D, F)),
            "w_up": _w((lambda *s: torch.randn(*s, dtype=torch.bfloat16) * 0.05)(D, F)),
            "w_down": _w((lambda *s: torch.randn(*s, dtype=torch.bfloat16) * 0.05)(F, D)),
        }
    _B_in = _B_setup()

    return {
        "A_q": _A_in["q"],
        "A_k": _A_in["k"],
        "A_v": _A_in["v"],
        "A_shift_k": _A_in["shift_k"],
        "A_ssm_k": _A_in["ssm_k"],
        "A_ssm_D": _A_in["ssm_D"],
        "B_x": _B_in["x"],
        "B_w_r": _B_in["w_r"],
        "B_w_gate": _B_in["w_gate"],
        "B_w_up": _B_in["w_up"],
        "B_w_down": _B_in["w_down"],
    }

def reference(A_q, A_k, A_v, A_shift_k, A_ssm_k, A_ssm_D, B_x, B_w_r, B_w_gate, B_w_up, B_w_down):
    def _A_ref(q, k, v, shift_k, ssm_k, ssm_D):
        B, S, D = q.shape
        ks = shift_k.shape[-1]
        k_pad = _pad(k, ((0, 0), (ks - 1, 0), (0, 0)))
        k_shift = torch.zeros_like(k)
        for t in range(ks):
            k_shift = k_shift + k_pad[:, t:t + S, :] * shift_k[:, ks - 1 - t][None, None, :]
        kv = k_shift * v
        L = 2 * S
        kv_pad = _pad(kv, ((0, 0), (0, S), (0, 0)))
        kf_pad = _pad(ssm_k, ((0, 0), (0, S)))
        kv_f = (lambda x, axes=(-2,-1): torch.fft.rfft2(x.float(), dim=axes))(kv_pad[:, None, :, :], axes=(1, 2))
        kf_f = (lambda x, axes=(-2,-1): torch.fft.rfft2(x.float(), dim=axes))(kf_pad[:, None, :], axes=(1, 2))
        kf_f = (lambda x, *d: x.permute(*d))(kf_f, 1, 2, 0)
        y_f = kv_f * kf_f[None, :, :, :]
        y = (lambda x, s, axes=(-2,-1): torch.fft.irfft2(x, s=s, dim=axes).to(torch.bfloat16))(y_f, (1, L), axes=(1, 2))[:, 0, :S, :]
        y = y + kv * ssm_D[None, None, :]
        return q * y

    def _B_ref(x, w_r, w_gate, w_up, w_down):
        B, S, D = x.shape
        C = 2
        r = x @ w_r
        idx = torch.argsort(-r, axis=-1)[:, :C]
        idx_e = (lambda x, n, axis: torch.repeat_interleave(x, n, dim=axis))(idx.reshape(B, C, 1), D, axis=2)
        x_sel = (lambda x, i, axis: torch.take_along_dim(x, i, dim=axis))(x, idx_e, axis=1)
        g = _silu(x_sel @ w_gate)
        u = x_sel @ w_up
        y = (g * u) @ w_down
        r_sel = _sigmoid((lambda x, i, axis: torch.take_along_dim(x, i, dim=axis))(r, idx, axis=1))
        y = y * r_sel.reshape(B, C, 1)
        b_off = (torch.arange(B) * S).reshape(B, 1)
        flat_idx = (idx + b_off).reshape(B * C)
        zero = (x * 0.0).reshape(B * S, D)
        upd = _scatter_add(zero, flat_idx, y.reshape(B * C, D), axis=0)
        return x + upd.reshape(B, S, D)

    x = A_q
    xn = _layernorm(x, axis=-1)
    return x + _A_ref(xn, A_k, A_v, A_shift_k, A_ssm_k, A_ssm_D) + _B_ref(xn, B_w_r, B_w_gate, B_w_up, B_w_down)
```

```cpp
#include <hip/hip_runtime.h>
#include <hip/hip_bf16.h>
#include <stdint.h>
typedef unsigned short u16;
constexpr int S = 2048, D = 640, F = 1728;
__device__ __forceinline__ float b2f(u16 x) { return __uint_as_float(((unsigned)x) << 16); }
__device__ __forceinline__ u16 f2b(float f) { __hip_bfloat16 h = __float2bfloat16(f); return *(u16*)&h; }
__device__ __forceinline__ float rb(float f) { return b2f(f2b(f)); }

__global__ void k1(const u16* q, const u16* k, const u16* v, const u16* sk, const u16* wr,
                   float* stats, float* r, u16* kvT) {
    int t = blockIdx.x; int tid = threadIdx.x;
    float x[10]; float s = 0;
    for (int i = 0; i < 10; i++) { x[i] = b2f(q[t * D + tid + 64 * i]); s += x[i]; }
    for (int m = 32; m; m >>= 1) s += __shfl_xor(s, m);
    float mean = s / D; float vs = 0;
    for (int i = 0; i < 10; i++) { float d = x[i] - mean; vs += d * d; }
    for (int m = 32; m; m >>= 1) vs += __shfl_xor(vs, m);
    float rstd = 1.0f / sqrtf(vs / D + 1e-5f);
    float rr = 0;
    for (int i = 0; i < 10; i++) rr += rb((x[i] - mean) * rstd) * b2f(wr[tid + 64 * i]);
    for (int m = 32; m; m >>= 1) rr += __shfl_xor(rr, m);
    if (tid == 0) { stats[2 * t] = mean; stats[2 * t + 1] = rstd; r[t] = rr; }
    for (int i = 0; i < 10; i++) {
        int d = tid + 64 * i; float a = 0;
        for (int j = 0; j < 4; j++) if (t - j >= 0) a += b2f(k[(t - j) * D + d]) * b2f(sk[d * 4 + j]);
        kvT[d * S + t] = f2b(a * b2f(v[t * D + d]));
    }
}
__global__ void k2(const u16* q, const u16* h, const u16* Dv, const float* stats, const u16* kvT, u16* out) {
    __shared__ float hs[S], us[S];
    int d = blockIdx.x; int tid = threadIdx.x;
    for (int i = tid; i < S; i += 256) { hs[i] = b2f(h[d * S + i]); us[i] = b2f(kvT[d * S + i]); }
    __syncthreads();
    float dd = b2f(Dv[d]);
    for (int t = tid; t < S; t += 256) {
        float a = 0;
        for (int s = 0; s <= t; s++) a += hs[s] * us[t - s];
        float y = a + us[t] * dd;
        float x = b2f(q[t * D + d]); float xn = (x - stats[2 * t]) * stats[2 * t + 1];
        out[t * D + d] = f2b(x + xn * y + xn);
    }
}
__device__ void top2(const float* r, int tid, int* i1, int* i2) {
    float a = -INFINITY, b = -INFINITY; int ia = 0, ib = 1;
    for (int i = 0; i < S; i++) { float x = r[i]; if (x > a) { b = a; ib = ia; a = x; ia = i; } else if (x > b) { b = x; ib = i; } }
    *i1 = ia; *i2 = ib;
}
__global__ void k3(const u16* q, const float* stats, const float* r, const u16* wg, const u16* wu, float* hbuf) {
    int i1, i2; top2(r, threadIdx.x, &i1, &i2);
    int f = blockIdx.x * 64 + threadIdx.x;
    float g0 = 0, g1 = 0, u0 = 0, u1 = 0;
    for (int d = 0; d < D; d++) {
        float x0 = rb((b2f(q[i1 * D + d]) - stats[2 * i1]) * stats[2 * i1 + 1]);
        float x1 = rb((b2f(q[i2 * D + d]) - stats[2 * i2]) * stats[2 * i2 + 1]);
        float a = b2f(wg[d * F + f]), b = b2f(wu[d * F + f]);
        g0 += x0 * a; g1 += x1 * a; u0 += x0 * b; u1 += x1 * b;
    }
    hbuf[f] = g0 / (1 + __expf(-g0)) * u0; hbuf[F + f] = g1 / (1 + __expf(-g1)) * u1;
}
__global__ void k4(const float* r, const u16* wd, const float* hbuf, u16* out) {
    int i1, i2; top2(r, threadIdx.x, &i1, &i2);
    int d = blockIdx.x * 64 + threadIdx.x;
    float y0 = 0, y1 = 0;
    for (int f = 0; f < F; f++) { float w = b2f(wd[f * D + d]); y0 += hbuf[f] * w; y1 += hbuf[F + f] * w; }
    y0 *= 1.0f / (1 + __expf(-r[i1])); y1 *= 1.0f / (1 + __expf(-r[i2]));
    out[i1 * D + d] = f2b(b2f(out[i1 * D + d]) + y0);
    out[i2 * D + d] = f2b(b2f(out[i2 * D + d]) + y1);
}
extern "C" void kernel_launch(void* const* d_in, const int* in_sizes, int n_in, void* d_out, int out_size, void* d_ws, size_t ws_size, hipStream_t stream) {
    const u16* q = (const u16*)d_in[0]; const u16* k = (const u16*)d_in[1]; const u16* v = (const u16*)d_in[2];
    const u16* sk = (const u16*)d_in[3]; const u16* h = (const u16*)d_in[4]; const u16* Dv = (const u16*)d_in[5];
    const u16* wr = (const u16*)d_in[7]; const u16* wg = (const u16*)d_in[8]; const u16* wu = (const u16*)d_in[9]; const u16* wd = (const u16*)d_in[10];
    u16* out = (u16*)d_out;
    char* ws = (char*)d_ws;
    float* stats = (float*)ws; float* r = (float*)(ws + 16384); float* hbuf = (float*)(ws + 32768); u16* kvT = (u16*)(ws + 65536);
    k1<<<S, 64, 0, stream>>>(q, k, v, sk, wr, stats, r, kvT);
    k2<<<D, 256, 0, stream>>>(q, h, Dv, stats, kvT, out);
    k3<<<F / 64, 64, 0, stream>>>(q, stats, r, wg, wu, hbuf);
    k4<<<D / 64, 64, 0, stream>>>(r, wd, hbuf, out);
}
```

```cpp
#include <hip/hip_runtime.h>
#include <hip/hip_bf16.h>
#include <stdint.h>
typedef unsigned short u16;
constexpr int S = 2048, D = 640, F = 1728;
__device__ __forceinline__ float b2f(u16 x) { return __uint_as_float(((unsigned)x) << 16); }
__device__ __forceinline__ u16 f2b(float f) { __hip_bfloat16 h = __float2bfloat16(f); return *(u16*)&h; }
__device__ __forceinline__ float rb(float f) { return b2f(f2b(f)); }

__global__ void k1(const u16* q, const u16* k, const u16* v, const u16* sk, const u16* wr,
                   float* stats, float* r, u16* kvT) {
    int t = blockIdx.x; int tid = threadIdx.x;
    float x[10]; float s = 0;
    for (int i = 0; i < 10; i++) { x[i] = b2f(q[t * D + tid + 64 * i]); s += x[i]; }
    for (int m = 32; m; m >>= 1) s += __shfl_xor(s, m);
    float mean = s / D; float vs = 0;
    for (int i = 0; i < 10; i++) { float d = x[i] - mean; vs += d * d; }
    for (int m = 32; m; m >>= 1) vs += __shfl_xor(vs, m);
    float rstd = 1.0f / sqrtf(vs / D + 1e-5f);
    float rr = 0;
    for (int i = 0; i < 10; i++) rr += rb((x[i] - mean) * rstd) * b2f(wr[tid + 64 * i]);
    for (int m = 32; m; m >>= 1) rr += __shfl_xor(rr, m);
    if (tid == 0) { stats[2 * t] = mean; stats[2 * t + 1] = rstd; r[t] = rr; }
    for (int i = 0; i < 10; i++) {
        int d = tid + 64 * i; float a = 0;
        for (int j = 0; j < 4; j++) if (t - j >= 0) a += b2f(k[(t - j) * D + d]) * b2f(sk[d * 4 + j]);
        kvT[d * S + t] = f2b(a * b2f(v[t * D + d]));
    }
}
__global__ void k2(const u16* q, const u16* h, const u16* Dv, const float* stats, const u16* kvT, u16* out) {
    __shared__ float hs[S], us[S];
    int d = blockIdx.x; int tid = threadIdx.x;
    for (int i = tid; i < S; i += 256) { hs[i] = b2f(h[d * S + i]); us[i] = b2f(kvT[d * S + i]); }
    __syncthreads();
    float dd = b2f(Dv[d]);
    for (int t = tid; t < S; t += 256) {
        float a = 0;
        for (int s = 0; s <= t; s++) a += hs[s] * us[t - s];
        float y = a + us[t] * dd;
        float x = b2f(q[t * D + d]); float xn = (x - stats[2 * t]) * stats[2 * t + 1];
        out[t * D + d] = f2b(x + xn * y + xn);
    }
}
__device__ void top2(const float* r, int tid, int* i1, int* i2) {
    float a = -INFINITY, b = -INFINITY; int ia = 0, ib = 1;
    for (int i = 0; i < S; i++) { float x = r[i]; if (x > a) { b = a; ib = ia; a = x; ia = i; } else if (x > b) { b = x; ib = i; } }
    *i1 = ia; *i2 = ib;
}
__global__ void k3(const u16* q, const float* stats, const float* r, const u16* wg, const u16* wu, float* hbuf) {
    int i1, i2; top2(r, threadIdx.x, &i1, &i2);
    int f = blockIdx.x * 64 + threadIdx.x;
    float g0 = 0, g1 = 0, u0 = 0, u1 = 0;
    for (int d = 0; d < D; d++) {
        float x0 = rb((b2f(q[i1 * D + d]) - stats[2 * i1]) * stats[2 * i1 + 1]);
        float x1 = rb((b2f(q[i2 * D + d]) - stats[2 * i2]) * stats[2 * i2 + 1]);
        float a = b2f(wg[d * F + f]), b = b2f(wu[d * F + f]);
        g0 += x0 * a; g1 += x1 * a; u0 += x0 * b; u1 += x1 * b;
    }
    hbuf[f] = g0 / (1 + __expf(-g0)) * u0; hbuf[F + f] = g1 / (1 + __expf(-g1)) * u1;
}
__global__ void k4(const float* r, const u16* wd, const float* hbuf, u16* out) {
    int i1, i2; top2(r, threadIdx.x, &i1, &i2);
    int d = blockIdx.x * 64 + threadIdx.x;
    float y0 = 0, y1 = 0;
    for (int f = 0; f < F; f++) { float w = b2f(wd[f * D + d]); y0 += hbuf[f] * w; y1 += hbuf[F + f] * w; }
    y0 *= 1.0f / (1 + __expf(-r[i1])); y1 *= 1.0f / (1 + __expf(-r[i2]));
    out[i1 * D + d] = f2b(b2f(out[i1 * D + d]) + y0);
    out[i2 * D + d] = f2b(b2f(out[i2 * D + d]) + y1);
}

typedef unsigned long long u64;
__device__ __forceinline__ u64 rt() { return __builtin_amdgcn_s_memrealtime(); }
__device__ __forceinline__ u64 ct() { u64 t; asm volatile("s_memtime %0\n\ts_waitcnt lgkmcnt(0)" : "=s"(t) :: "memory"); return t; }
#define R4(x) x x x x
#define R16(x) R4(R4(x))
#define R256(x) R16(R16(x))
#define NREP(x) x
__global__ void __launch_bounds__(256) probe(float* sink, int n) {
    float a = threadIdx.x, b = 1.0001f;
    u64 d0 = 0, d1 = 0;
    for (int it = 0; it < n; it++) {
        u64 t0 = ct();
        asm volatile(NREP(R256("v_fma_f32 %0, %0, %1, %1\n\t")) : "+v"(a) : "v"(b));
        u64 t1 = ct();
        if (it == 0) d0 = t1 - t0; else d1 = t1 - t0;
    }
    if (a == 1.2345f) sink[0] = a;
    u64 d = d0 > d1 ? d0 - d1 : 0; if (d > 20000) d = 20000;
    u64 until = rt() + 10 * d;
    while (rt() < until) __builtin_amdgcn_s_sleep(8);
}
extern "C" void kernel_launch(void* const* d_in, const int* in_sizes, int n_in, void* d_out, int out_size, void* d_ws, size_t ws_size, hipStream_t stream) {
    const u16* q = (const u16*)d_in[0]; const u16* k = (const u16*)d_in[1]; const u16* v = (const u16*)d_in[2];
    const u16* sk = (const u16*)d_in[3]; const u16* h = (const u16*)d_in[4]; const u16* Dv = (const u16*)d_in[5];
    const u16* wr = (const u16*)d_in[7]; const u16* wg = (const u16*)d_in[8]; const u16* wu = (const u16*)d_in[9]; const u16* wd = (const u16*)d_in[10];
    u16* out = (u16*)d_out;
    char* ws = (char*)d_ws;
    float* stats = (float*)ws; float* r = (float*)(ws + 16384); float* hbuf = (float*)(ws + 32768); u16* kvT = (u16*)(ws + 65536);
    probe<<<256, 256, 0, stream>>>((float*)(ws + (64<<20)), 2);
    k1<<<S, 64, 0, stream>>>(q, k, v, sk, wr, stats, r, kvT);
    k2<<<D, 256, 0, stream>>>(q, h, Dv, stats, kvT, out);
    k3<<<F / 64, 64, 0, stream>>>(q, stats, r, wg, wu, hbuf);
    k4<<<D / 64, 64, 0, stream>>>(r, wd, hbuf, out);
}
```

```cpp
#include <hip/hip_runtime.h>
typedef unsigned u32; typedef unsigned long long u64; typedef unsigned short u16;
typedef u32 v4u __attribute__((ext_vector_type(4)));
constexpr int S = 2048, D = 640, F = 1728;
#define BUF const void*, u32, u32
__global__ void __launch_bounds__(512, 2) fused(BUF, BUF, BUF, BUF, BUF, BUF, BUF, BUF, BUF, BUF, BUF, BUF) {
#if defined(__HIP_DEVICE_COMPILE__)
    __shared__ __attribute__((aligned(16))) char smem[156640];
    u32 tid = __builtin_amdgcn_workitem_id_x();
    asm volatile(
    "s_load_dwordx16 s[8:23], s[0:1], 0x0\n"
    "s_load_dwordx16 s[24:39], s[0:1], 0x40\n"
    "s_load_dwordx16 s[40:55], s[0:1], 0x80\n"
    "s_memrealtime s[100:101]\n"
    "s_getpc_b64 s[4:5]\n"
    "Lpc:\n"
    "s_and_b32 s6, s2, -8\n"
    "v_lshrrev_b32 v1, 6, v0\n"
    "v_add_u32 v1, s6, v1\n"
    "v_min_u32 v1, (Lend-Lpc-4)/128, v1\n"
    "s_mov_b32 m0, 156384\n"
    "v_lshlrev_b32 v1, 7, v1\n"
    "global_load_lds_dword v1, s[4:5]\n"
    "v_readfirstlane_b32 s3, v0\n"
    "v_and_b32 v1, 63, v0\n"
    "s_and_b32 s60, s2, 7\n"
    "s_lshr_b32 s61, s2, 3\n"
    "s_lshr_b32 s3, s3, 6\n"
    "s_sub_u32 s65, s2, 160\n"
    "s_cbranch_scc1 Lconv\n"
    "s_cmp_ge_u32 s3, 4\n"
    "s_cbranch_scc1 Lstaty\n"
    "s_and_b32 s70, s3, 3\n"
    "s_lshl_b32 s4, s65, 2\n"
    "s_add_u32 s71, s4, s70\n"
    "s_mul_i32 s72, s71, 5120\n"
    "v_lshrrev_b32 v2, 4, v1\n"
    "v_and_b32 v3, 15, v1\n"
    "v_mul_u32_u24 v4, 1280, v2\n"
    "v_lshl_or_b32 v4, v3, 4, v4\n"
    "v_lshlrev_b32 v5, 4, v0\n"
    "s_waitcnt lgkmcnt(0)\n"
    "buffer_load_dwordx4 v[18:21], v5, s[40:43], 0 offen\n"
    "buffer_load_dwordx4 v[22:25], v4, s[32:35], s72 offen offset:0\n"
    "buffer_load_dwordx4 v[26:29], v4, s[32:35], s72 offen offset:256\n"
    "buffer_load_dwordx4 v[30:33], v4, s[32:35], s72 offen offset:512\n"
    "buffer_load_dwordx4 v[34:37], v4, s[32:35], s72 offen offset:768\n"
    "buffer_load_dwordx4 v[38:41], v4, s[32:35], s72 offen offset:1024\n"
    "s_load_dwordx2 s[56:57], s[20:21], 0x0\n"
    "s_branch Lstat\n"
    "Lconv:\n"
    "s_mul_i32 s62, s60, 20\n"
    "s_add_u32 s62, s62, s61\n"
    "s_cmp_ge_u32 s3, 4\n"
    "s_cbranch_scc1 Lmat\n"
    "s_lshr_b32 s63, s62, 4\n"
    "s_and_b32 s64, s62, 15\n"
    "s_lshl_b32 s4, s3, 4\n"
    "s_add_u32 s4, s4, s64\n"
    "s_mul_i32 s65, s4, 20480\n"
    "s_add_u32 s60, s63, 5\n"
    "s_sub_u32 s5, s60, 10\n"
    "s_cselect_b32 s60, s60, s5\n"
    "s_lshl_b32 s5, s60, 7\n"
    "s_add_u32 s65, s65, s5\n"
    "s_add_u32 s66, s65, 10240\n"
    "s_sub_u32 s67, s65, 10240\n"
    "s_cselect_b32 s67, 0, s67\n"
    "s_cselect_b32 s5, 2147483648, 0\n"
    "v_lshrrev_b32 v2, 3, v1\n"
    "v_and_b32 v3, 7, v1\n"
    "v_mul_u32_u24 v4, 1280, v2\n"
    "v_lshl_or_b32 v4, v3, 4, v4\n"
    "v_cmp_lt_u32 vcc, 39, v1\n"
    "v_bfrev_b32 v8, 1\n"
    "v_lshlrev_b32 v7, 3, v1\n"
    "v_cndmask_b32 v5, v8, v4, vcc\n"
    "s_lshl_b32 s4, s60, 9\n"
    "v_or_b32 v6, s5, v5\n"
    "s_waitcnt lgkmcnt(0)\n"
    "buffer_load_dwordx4 v[10:13], v6, s[8:11], s67 offen\n"
    "buffer_load_dwordx4 v[14:17], v4, s[8:11], s65 offen nt\n"
    "buffer_load_dwordx4 v[18:21], v4, s[8:11], s66 offen\n"
    "buffer_load_dwordx4 v[22:25], v4, s[12:15], s65 offen nt\n"
    "buffer_load_dwordx4 v[26:29], v4, s[12:15], s66 offen nt\n"
    "buffer_load_dwordx2 v[50:51], v7, s[16:19], s4 offen\n"
    "s_load_dwordx2 s[56:57], s[20:21], 0x0\n"
    "v_lshlrev_b32 v52, 4, v1\n"
    "s_add_u32 s65, s65, 1310720\n"
    "s_add_u32 s66, s66, 1310720\n"
    "s_sub_u32 s67, s65, 10240\n"
    "v_and_b32 v69, 3, v1\n"
    "s_mov_b32 s6, 2027808452\n"
    "s_mov_b32 s7, 2246822519\n"
    "v_mul_lo_u32 v71, v69, s6\n"
    "v_add_u32 v74, 2654435761, v71\n"
    "v_add_u32 v75, 1013904226, v71\n"
    "v_add_u32 v76, 3668339987, v71\n"
    "v_add_u32 v77, 2027808452, v71\n"
    "v_lshrrev_b32 v71, 15, v74\n"
    "v_bitop3_b32 v74, v74, v71, 1 bitop3:0xbe\n"
    "v_lshrrev_b32 v71, 15, v75\n"
    "v_bitop3_b32 v75, v75, v71, 1 bitop3:0xbe\n"
    "v_lshrrev_b32 v71, 15, v76\n"
    "v_bitop3_b32 v76, v76, v71, 1 bitop3:0xbe\n"
    "v_lshrrev_b32 v71, 15, v77\n"
    "v_bitop3_b32 v77, v77, v71, 1 bitop3:0xbe\n"
    "v_mul_lo_u32 v78, v74, s7\n"
    "v_mul_lo_u32 v79, v75, s7\n"
    "v_mul_lo_u32 v80, v76, s7\n"
    "v_mul_lo_u32 v81, v77, s7\n"
    "s_lshl_b32 s4, s60, 3\n"
    "s_add_u32 s4, s4, s3\n"
    "s_lshl_b32 s4, s4, 4\n"
    "s_add_u32 s4, s4, s64\n"
    "s_mul_i32 s69, s4, 2176\n"
    "s_add_u32 s69, s69, 354304\n"
    "v_and_b32 v9, 4, v1\n"
    "s_mul_i32 s4, s3, 5248\n"
    "s_add_u32 s4, s4, 126080\n"
    "v_mul_u32_u24 v53, 656, v9\n"
    "v_lshl_add_u32 v53, v2, 6, v53\n"
    "v_lshl_add_u32 v53, v69, 4, v53\n"
    "v_add_u32 v53, s4, v53\n"
    "v_lshrrev_b32 v54, 5, v1\n"
    "v_mul_u32_u24 v54, 2624, v54\n"
    "v_bfe_u32 v71, v1, 2, 2\n"
    "v_lshl_add_u32 v54, v71, 6, v54\n"
    "v_bfe_u32 v71, v1, 4, 1\n"
    "v_lshl_add_u32 v54, v71, 5, v54\n"
    "v_lshl_add_u32 v54, v69, 3, v54\n"
    "v_add_u32 v54, s4, v54\n"
    "v_lshl_add_u32 v72, v2, 7, v52\n"
    "v_cmp_eq_u32 s[76:77], 0, v69\n"
    "v_lshlrev_b32 v56, 1, v1\n"
    "s_cmp_eq_u32 s3, 3\n"
    "s_cselect_b32 s4, 8, 0\n"
    "v_cndmask_b32 v56, v8, v56, s[76:77]\n"
    "v_cmp_gt_u32 vcc, s4, v1\n"
    "s_waitcnt lgkmcnt(0)\n"
    "s_mul_i32 s58, s56, 0x583678da\n"
    "s_mul_i32 s59, s57, 0xbe94f82a\n"
    "s_add_u32 s58, s58, 0x52f45a73\n"
    "s_add_u32 s59, s59, 0x370e6b27\n"
    "s_barrier\n"
    "v_cndmask_b32 v67, v8, v52, vcc\n"
    "v_mov_b32 v86, s58\n"
    "v_mov_b32 v87, s59\n"
    "v_mov_b32 v88, s58\n"
    "v_mov_b32 v89, s59\n"
    "v_cndmask_b32 v90, 0, v86, s[76:77]\n"
    "v_cndmask_b32 v91, 0, v87, s[76:77]\n"
    "s_waitcnt vmcnt(5)\n"
    "ds_write_b128 v53, v[10:13] offset:0\n"
    "s_waitcnt vmcnt(4)\n"
    "ds_write_b128 v53, v[14:17] offset:512\n"
    "s_waitcnt vmcnt(3)\n"
    "ds_write_b128 v53, v[18:21] offset:1024\n"
    "s_waitcnt vmcnt(2)\n"
    "ds_write_b128 v53, v[22:25] offset:1536\n"
    "s_waitcnt vmcnt(1)\n"
    "ds_write_b128 v53, v[26:29] offset:2048\n"
    "ds_read_b64_tr_b16 v[94:95], v54 offset:320\n"
    "ds_read_b64_tr_b16 v[96:97], v54 offset:384\n"
    "ds_read_b64_tr_b16 v[98:99], v54 offset:576\n"
    "ds_read_b64_tr_b16 v[100:101], v54 offset:640\n"
    "ds_read_b64_tr_b16 v[114:115], v54 offset:1536\n"
    "ds_read_b64_tr_b16 v[102:103], v54 offset:832\n"
    "ds_read_b64_tr_b16 v[104:105], v54 offset:896\n"
    "ds_read_b64_tr_b16 v[116:117], v54 offset:1792\n"
    "ds_read_b64_tr_b16 v[106:107], v54 offset:1088\n"
    "ds_read_b64_tr_b16 v[108:109], v54 offset:1152\n"
    "ds_read_b64_tr_b16 v[118:119], v54 offset:2048\n"
    "ds_read_b64_tr_b16 v[110:111], v54 offset:1344\n"
    "ds_read_b64_tr_b16 v[112:113], v54 offset:1408\n"
    "ds_read_b64_tr_b16 v[120:121], v54 offset:2304\n"
    "s_waitcnt vmcnt(0)\n"
    "v_alignbit_b32 v50, v50, v50, 16\n"
    "v_alignbit_b32 v51, v51, v51, 16\n"
    "s_waitcnt lgkmcnt(13)\n"
    "v_dot2_f32_bf16 v122, v94, v51, 0\n"
    "s_waitcnt lgkmcnt(12)\n"
    "v_dot2_f32_bf16 v123, v96, v51, 0\n"
    "v_dot2_f32_bf16 v124, v95, v51, 0\n"
    "v_dot2_f32_bf16 v125, v97, v51, 0\n"
    "s_waitcnt lgkmcnt(11)\n"
    "v_dot2_f32_bf16 v126, v98, v51, 0\n"
    "s_waitcnt lgkmcnt(10)\n"
    "v_dot2_f32_bf16 v127, v100, v51, 0\n"
    "v_dot2_f32_bf16 v128, v99, v51, 0\n"
    "v_dot2_f32_bf16 v129, v101, v51, 0\n"
    "v_dot2_f32_bf16 v122, v95, v50, v122\n"
    "v_dot2_f32_bf16 v123, v97, v50, v123\n"
    "v_dot2_f32_bf16 v124, v98, v50, v124\n"
    "v_dot2_f32_bf16 v125, v100, v50, v125\n"
    "v_dot2_f32_bf16 v126, v99, v50, v126\n"
    "v_dot2_f32_bf16 v127, v101, v50, v127\n"
    "s_waitcnt lgkmcnt(8)\n"
    "v_dot2_f32_bf16 v128, v102, v50, v128\n"
    "s_waitcnt lgkmcnt(7)\n"
    "v_dot2_f32_bf16 v129, v104, v50, v129\n"
    "v_lshlrev_b32 v138, 16, v114\n"
    "v_and_b32 v139, 0xffff0000, v114\n"
    "v_lshlrev_b32 v140, 16, v115\n"
    "v_and_b32 v141, 0xffff0000, v115\n"
    "s_waitcnt lgkmcnt(6)\n"
    "v_lshlrev_b32 v142, 16, v116\n"
    "v_and_b32 v143, 0xffff0000, v116\n"
    "v_lshlrev_b32 v144, 16, v117\n"
    "v_and_b32 v145, 0xffff0000, v117\n"
    "v_pk_mul_f32 v[122:123], v[122:123], v[138:139]\n"
    "v_pk_mul_f32 v[124:125], v[124:125], v[140:141]\n"
    "v_pk_mul_f32 v[126:127], v[126:127], v[142:143]\n"
    "v_pk_mul_f32 v[128:129], v[128:129], v[144:145]\n"
    "v_dot2_f32_bf16 v130, v102, v51, 0\n"
    "v_dot2_f32_bf16 v131, v104, v51, 0\n"
    "v_dot2_f32_bf16 v132, v103, v51, 0\n"
    "v_dot2_f32_bf16 v133, v105, v51, 0\n"
    "s_waitcnt lgkmcnt(5)\n"
    "v_dot2_f32_bf16 v134, v106, v51, 0\n"
    "s_waitcnt lgkmcnt(4)\n"
    "v_dot2_f32_bf16 v135, v108, v51, 0\n"
    "v_dot2_f32_bf16 v136, v107, v51, 0\n"
    "v_dot2_f32_bf16 v137, v109, v51, 0\n"
    "v_dot2_f32_bf16 v130, v103, v50, v130\n"
    "v_dot2_f32_bf16 v131, v105, v50, v131\n"
    "v_dot2_f32_bf16 v132, v106, v50, v132\n"
    "v_dot2_f32_bf16 v133, v108, v50, v133\n"
    "v_dot2_f32_bf16 v134, v107, v50, v134\n"
    "v_dot2_f32_bf16 v135, v109, v50, v135\n"
    "s_waitcnt lgkmcnt(2)\n"
    "v_dot2_f32_bf16 v136, v110, v50, v136\n"
    "s_waitcnt lgkmcnt(1)\n"
    "v_dot2_f32_bf16 v137, v112, v50, v137\n"
    "v_lshlrev_b32 v146, 16, v118\n"
    "v_and_b32 v147, 0xffff0000, v118\n"
    "v_lshlrev_b32 v148, 16, v119\n"
    "v_and_b32 v149, 0xffff0000, v119\n"
    "s_waitcnt lgkmcnt(0)\n"
    "v_lshlrev_b32 v150, 16, v120\n"
    "v_and_b32 v151, 0xffff0000, v120\n"
    "v_lshlrev_b32 v152, 16, v121\n"
    "v_and_b32 v153, 0xffff0000, v121\n"
    "v_pk_mul_f32 v[130:131], v[130:131], v[146:147]\n"
    "v_pk_mul_f32 v[132:133], v[132:133], v[148:149]\n"
    "v_pk_mul_f32 v[134:135], v[134:135], v[150:151]\n"
    "v_pk_mul_f32 v[136:137], v[136:137], v[152:153]\n"
    "v_cvt_pk_bf16_f32 v154, v129, v128\n"
    "v_cvt_pk_bf16_f32 v155, v127, v126\n"
    "v_cvt_pk_bf16_f32 v156, v125, v124\n"
    "v_cvt_pk_bf16_f32 v157, v123, v122\n"
    "v_cvt_pk_bf16_f32 v158, v137, v136\n"
    "v_cvt_pk_bf16_f32 v159, v135, v134\n"
    "v_cvt_pk_bf16_f32 v160, v133, v132\n"
    "v_cvt_pk_bf16_f32 v161, v131, v130\n"
    "v_mad_u64_u32 v[162:163], s[6:7], v154, v74, v[90:91]\n"
    "v_mad_u64_u32 v[164:165], s[6:7], v158, v78, 0\n"
    "v_mad_u64_u32 v[162:163], s[6:7], v155, v75, v[162:163]\n"
    "v_mad_u64_u32 v[164:165], s[6:7], v159, v79, v[164:165]\n"
    "v_mad_u64_u32 v[162:163], s[6:7], v156, v76, v[162:163]\n"
    "v_mad_u64_u32 v[164:165], s[6:7], v160, v80, v[164:165]\n"
    "v_mad_u64_u32 v[162:163], s[6:7], v157, v77, v[162:163]\n"
    "v_mad_u64_u32 v[164:165], s[6:7], v161, v81, v[164:165]\n"
    "v_add_u32 v166, v162, v164\n"
    "v_add_u32 v167, v163, v165\n"
    "v_mov_b32 v138, v154\n"
    "v_mov_b32 v139, v155\n"
    "v_mov_b32 v140, v156\n"
    "v_mov_b32 v141, v157\n"
    "v_add_u32_dpp v166, v166, v166 quad_perm:[1,0,3,2] row_mask:0xf bank_mask:0xf\n"
    "v_add_u32_dpp v167, v167, v167 quad_perm:[1,0,3,2] row_mask:0xf bank_mask:0xf\n"
    "v_mov_b32_dpp v154, v158 row_shr:4 row_mask:0xf bank_mask:0xa\n"
    "v_mov_b32_dpp v155, v159 row_shr:4 row_mask:0xf bank_mask:0xa\n"
    "v_mov_b32_dpp v156, v160 row_shr:4 row_mask:0xf bank_mask:0xa\n"
    "v_mov_b32_dpp v157, v161 row_shr:4 row_mask:0xf bank_mask:0xa\n"
    "v_add_u32_dpp v166, v166, v166 quad_perm:[2,3,0,1] row_mask:0xf bank_mask:0xf\n"
    "v_add_u32_dpp v167, v167, v167 quad_perm:[2,3,0,1] row_mask:0xf bank_mask:0xf\n"
    "buffer_store_dwordx4 v[154:157], v72, s[20:23], s69 offen sc1\n"
    "buffer_store_dwordx2 v[166:167], v56, s[20:23], s69 offen offset:2048 sc1\n"
    "v_mov_b32_dpp v158, v138 row_shl:4 row_mask:0xf bank_mask:0x5\n"
    "v_mov_b32_dpp v159, v139 row_shl:4 row_mask:0xf bank_mask:0x5\n"
    "v_mov_b32_dpp v160, v140 row_shl:4 row_mask:0xf bank_mask:0x5\n"
    "v_mov_b32_dpp v161, v141 row_shl:4 row_mask:0xf bank_mask:0x5\n"
    "buffer_store_dwordx4 v[158:161], v72, s[20:23], s69 offen offset:128 sc1\n"
    "buffer_load_dwordx4 v[30:33], v5, s[8:11], s67 offen\n"
    "buffer_load_dwordx4 v[34:37], v4, s[8:11], s65 offen nt\n"
    "buffer_load_dwordx4 v[38:41], v4, s[8:11], s66 offen\n"
    "buffer_load_dwordx4 v[42:45], v4, s[12:15], s65 offen nt\n"
    "buffer_load_dwordx4 v[46:49], v4, s[12:15], s66 offen nt\n"
    "s_add_u32 s69, s69, 139264\n"
    "s_lshl_b32 s4, s2, 7\n"
    "s_add_u32 s4, s4, 4096\n"
    "buffer_store_dwordx4 v[86:89], v67, s[20:23], s4 offen sc1\n"
    "s_lshl_b32 s4, s63, 3\n"
    "s_add_u32 s4, s4, s3\n"
    "s_mul_i32 s68, s4, 34816\n"
    "s_add_u32 s68, s68, 354304\n"
    "s_lshr_b32 s75, s3, 1\n"
    "s_lshl_b32 s71, s3, 10\n"
    "s_add_u32 s71, s71, 61440\n"
    "s_mul_i32 s74, s3, 163840\n"
    "s_lshl_b32 s4, s62, 3\n"
    "s_add_u32 s74, s74, s4\n"
    "v_cmp_ne_u32 vcc, 0, v9\n"
    "s_lshl_b32 s4, s64, 7\n"
    "v_mul_u32_u24 v57, 2176, v2\n"
    "v_cndmask_b32 v82, v74, v78, vcc\n"
    "v_cndmask_b32 v83, v75, v79, vcc\n"
    "v_cndmask_b32 v84, v76, v80, vcc\n"
    "v_cndmask_b32 v85, v77, v81, vcc\n"
    "v_cmp_eq_u32 vcc, 0, v3\n"
    "v_lshl_add_u32 v58, v9, 1, v2\n"
    "v_mul_u32_u24 v58, 2176, v58\n"
    "v_cndmask_b32 v92, 0, v86, vcc\n"
    "v_cndmask_b32 v93, 0, v87, vcc\n"
    "v_lshl_add_u32 v71, v3, 4, s4\n"
    "v_add_u32 v57, v71, v57\n"
    "s_and_b32 s5, s3, 1\n"
    "s_lshl_b32 s5, s5, 3\n"
    "v_add_u32 v70, s5, v2\n"
    "s_lshl_b32 s4, s64, 3\n"
    "v_add_u32 v58, s4, v58\n"
    "v_mul_u32_u24 v59, 4640, v69\n"
    "v_lshl_add_u32 v59, v2, 5, v59\n"
    "v_lshl_add_u32 v59, v9, 2, v59\n"
    "s_lshl_b32 s4, s3, 9\n"
    "s_add_u32 s4, s4, 67040\n"
    "v_add_u32 v59, s4, v59\n"
    "v_lshlrev_b32 v62, 2, v1\n"
    "v_add_u32 v62, 152928, v62\n"
    "v_add_u32 v63, s3, v62\n"
    "v_lshrrev_b32 v64, 1, v3\n"
    "v_mul_u32_u24 v64, 320, v64\n"
    "v_lshl_add_u32 v64, v70, 4, v64\n"
    "v_and_b32 v71, 1, v1\n"
    "v_lshl_add_u32 v64, v71, 3, v64\n"
    "s_mul_i32 s4, s75, 1280\n"
    "s_add_u32 s4, s4, 85120\n"
    "v_add_u32 v64, s4, v64\n"
    "v_mul_u32_u24 v65, 2560, v1\n"
    "v_mov_b32 v68, 1\n"
    "s_add_u32 s78, s68, 17408\n"
    "s_add_u32 s79, s68, 139264\n"
    "s_add_u32 s80, s68, 156672\n"
    "s_add_u32 s90, s71, 8192\n"
    "s_add_u32 s91, s71, 12288\n"
    "s_sub_u32 s65, s65, 1310720\n"
    "s_sub_u32 s66, s66, 1310720\n"
    "s_add_u32 s74, s74, 1310720\n"
    "s_and_b32 s4, s2, 7\n"
    "s_xor_b32 s4, s4, 4\n"
    "s_lshl_b32 s4, s4, 1\n"
    "s_and_b32 s5, s61, 17\n"
    "s_add_u32 s4, s4, s5\n"
    "s_lshl_b32 s4, s4, 7\n"
    "s_add_u32 s75, s4, 53248\n"
    "s_lshl_b32 s4, s63, 2\n"
    "s_add_u32 s4, s4, s3\n"
    "s_lshl_b32 s4, s4, 4\n"
    "s_add_u32 s4, s4, s64\n"
    "s_mul_i32 s70, s4, 2176\n"
    "s_add_u32 s70, s70, 3139584\n"
    "s_lshl_b32 s4, s60, 2\n"
    "s_add_u32 s4, s4, s3\n"
    "s_mul_i32 s67, s4, 34816\n"
    "s_add_u32 s67, s67, 3139584\n"
    "s_add_u32 s81, s67, 17408\n"
    "s_lshl_b32 s4, s3, 4\n"
    "s_add_u32 s4, s4, s64\n"
    "s_mov_b32 s95, s4\n"
    "s_lshl_b32 s71, s4, 7\n"
    "s_add_u32 s71, s71, 61440\n"
    "s_lshl_b32 s4, s4, 4\n"
    "v_add_u32 v211, s4, v2\n"
    "s_and_b32 s4, s3, 1\n"
    "s_lshl_b32 s4, s4, 7\n"
    "s_lshr_b32 s5, s3, 1\n"
    "s_sub_u32 s5, s3, s5\n"
    "s_mul_i32 s5, s5, 1280\n"
    "s_sub_u32 s4, s5, s4\n"
    "v_add_u32 v206, s4, v64\n"
    "s_mul_i32 s4, s3, 5248\n"
    "s_add_u32 s4, s4, 126080\n"
    "v_mul_u32_u24 v207, 144, v3\n"
    "v_lshl_add_u32 v207, v2, 3, v207\n"
    "v_add_u32 v207, s4, v207\n"
    "v_lshrrev_b32 v209, 1, v2\n"
    "v_mul_u32_u24 v208, 144, v209\n"
    "v_lshlrev_b32 v209, 4, v209\n"
    "v_and_b32 v210, 1, v2\n"
    "v_mul_u32_u24 v210, 1168, v210\n"
    "v_add3_u32 v208, v208, v210, s4\n"
    "v_lshl_add_u32 v208, v3, 4, v208\n"
    "v_lshl_add_u32 v210, v9, 1, v2\n"
    "v_lshlrev_b32 v210, 3, v210\n"
    "v_cndmask_b32 v210, v8, v210, s[76:77]\n"
    "s_lshr_b32 s4, s3, 1\n"
    "s_mul_i32 s4, s4, 160\n"
    "s_add_u32 s4, s4, s63\n"
    "s_mul_i32 s92, s4, 2176\n"
    "s_add_u32 s92, s92, 4532224\n"
    "s_add_u32 s93, s92, 696320\n"
    "s_lshl_b32 s4, s64, 7\n"
    "v_mul_u32_u24 v60, 21760, v70\n"
    "v_lshl_add_u32 v61, v3, 4, s4\n"
    "v_add_u32 v60, v61, v60\n"
    "v_lshl_add_u32 v61, v9, 3, v70\n"
    "v_mul_u32_u24 v61, 21760, v61\n"
    "s_lshl_b32 s4, s64, 3\n"
    "v_add_u32 v61, s4, v61\n"
    "v_cmp_gt_u32 vcc, 4, v1\n"
    "v_lshlrev_b32 v55, 1, v69\n"
    "s_add_u32 s64, s74, 655360\n"
    "s_lshr_b32 s73, s62, 5\n"
    "s_lshl_b32 s73, s73, 1\n"
    "s_and_b32 s5, s62, 1\n"
    "s_add_u32 s73, s73, s5\n"
    "s_lshl_b32 s73, s73, 5\n"
    "s_bfe_u32 s5, s62, 0x40001\n"
    "s_add_u32 s73, s73, s5\n"
    "s_lshl_b32 s73, s73, 4\n"
    "s_add_u32 s73, s73, 77824\n"
    "v_cndmask_b32 v55, v8, v55, vcc\n"
    "v_cmp_gt_u32 vcc, 54, v1\n"
    "v_mul_u32_u24 v66, 5120, v1\n"
    "s_nop 0\n"
    "v_cndmask_b32 v66, v8, v66, vcc\n"
    "Lt1:\n"
    "s_sleep 1\n"
    "s_memrealtime s[6:7]\n"
    "s_waitcnt lgkmcnt(0)\n"
    "s_sub_u32 s5, s6, s100\n"
    "s_cmp_lt_u32 s5, 275\n"
    "s_cbranch_scc1 Lt1\n"
    "buffer_load_dwordx4 v[168:171], v57, s[20:23], s68 offen sc1\n"
    "buffer_load_dwordx4 v[172:175], v57, s[20:23], s78 offen sc1\n"
    "buffer_load_dwordx2 v[176:177], v58, s[20:23], s68 offen offset:2048 sc1\n"
    "s_waitcnt vmcnt(8)\n"
    "ds_write_b128 v53, v[30:33] offset:0\n"
    "s_waitcnt vmcnt(7)\n"
    "ds_write_b128 v53, v[34:37] offset:512\n"
    "s_waitcnt vmcnt(6)\n"
    "ds_write_b128 v53, v[38:41] offset:1024\n"
    "s_waitcnt vmcnt(5)\n"
    "ds_write_b128 v53, v[42:45] offset:1536\n"
    "s_waitcnt vmcnt(4)\n"
    "ds_write_b128 v53, v[46:49] offset:2048\n"
    "ds_read_b64_tr_b16 v[94:95], v54 offset:320\n"
    "ds_read_b64_tr_b16 v[96:97], v54 offset:384\n"
    "ds_read_b64_tr_b16 v[98:99], v54 offset:576\n"
    "ds_read_b64_tr_b16 v[100:101], v54 offset:640\n"
    "ds_read_b64_tr_b16 v[114:115], v54 offset:1536\n"
    "ds_read_b64_tr_b16 v[102:103], v54 offset:832\n"
    "ds_read_b64_tr_b16 v[104:105], v54 offset:896\n"
    "ds_read_b64_tr_b16 v[116:117], v54 offset:1792\n"
    "ds_read_b64_tr_b16 v[106:107], v54 offset:1088\n"
    "ds_read_b64_tr_b16 v[108:109], v54 offset:1152\n"
    "ds_read_b64_tr_b16 v[118:119], v54 offset:2048\n"
    "ds_read_b64_tr_b16 v[110:111], v54 offset:1344\n"
    "ds_read_b64_tr_b16 v[112:113], v54 offset:1408\n"
    "ds_read_b64_tr_b16 v[120:121], v54 offset:2304\n"
    "s_waitcnt lgkmcnt(13)\n"
    "v_dot2_f32_bf16 v122, v94, v51, 0\n"
    "s_waitcnt lgkmcnt(12)\n"
    "v_dot2_f32_bf16 v123, v96, v51, 0\n"
    "v_dot2_f32_bf16 v124, v95, v51, 0\n"
    "v_dot2_f32_bf16 v125, v97, v51, 0\n"
    "s_waitcnt lgkmcnt(11)\n"
    "v_dot2_f32_bf16 v126, v98, v51, 0\n"
    "s_waitcnt lgkmcnt(10)\n"
    "v_dot2_f32_bf16 v127, v100, v51, 0\n"
    "v_dot2_f32_bf16 v128, v99, v51, 0\n"
    "v_dot2_f32_bf16 v129, v101, v51, 0\n"
    "v_dot2_f32_bf16 v122, v95, v50, v122\n"
    "v_dot2_f32_bf16 v123, v97, v50, v123\n"
    "v_dot2_f32_bf16 v124, v98, v50, v124\n"
    "v_dot2_f32_bf16 v125, v100, v50, v125\n"
    "v_dot2_f32_bf16 v126, v99, v50, v126\n"
    "v_dot2_f32_bf16 v127, v101, v50, v127\n"
    "s_waitcnt lgkmcnt(8)\n"
    "v_dot2_f32_bf16 v128, v102, v50, v128\n"
    "s_waitcnt lgkmcnt(7)\n"
    "v_dot2_f32_bf16 v129, v104, v50, v129\n"
    "v_lshlrev_b32 v138, 16, v114\n"
    "v_and_b32 v139, 0xffff0000, v114\n"
    "v_lshlrev_b32 v140, 16, v115\n"
    "v_and_b32 v141, 0xffff0000, v115\n"
    "s_waitcnt lgkmcnt(6)\n"
    "v_lshlrev_b32 v142, 16, v116\n"
    "v_and_b32 v143, 0xffff0000, v116\n"
    "v_lshlrev_b32 v144, 16, v117\n"
    "v_and_b32 v145, 0xffff0000, v117\n"
    "v_pk_mul_f32 v[122:123], v[122:123], v[138:139]\n"
    "v_pk_mul_f32 v[124:125], v[124:125], v[140:141]\n"
    "v_pk_mul_f32 v[126:127], v[126:127], v[142:143]\n"
    "v_pk_mul_f32 v[128:129], v[128:129], v[144:145]\n"
    "v_dot2_f32_bf16 v130, v102, v51, 0\n"
    "v_dot2_f32_bf16 v131, v104, v51, 0\n"
    "v_dot2_f32_bf16 v132, v103, v51, 0\n"
    "v_dot2_f32_bf16 v133, v105, v51, 0\n"
    "s_waitcnt lgkmcnt(5)\n"
    "v_dot2_f32_bf16 v134, v106, v51, 0\n"
    "s_waitcnt lgkmcnt(4)\n"
    "v_dot2_f32_bf16 v135, v108, v51, 0\n"
    "v_dot2_f32_bf16 v136, v107, v51, 0\n"
    "v_dot2_f32_bf16 v137, v109, v51, 0\n"
    "v_dot2_f32_bf16 v130, v103, v50, v130\n"
    "v_dot2_f32_bf16 v131, v105, v50, v131\n"
    "v_dot2_f32_bf16 v132, v106, v50, v132\n"
    "v_dot2_f32_bf16 v133, v108, v50, v133\n"
    "v_dot2_f32_bf16 v134, v107, v50, v134\n"
    "v_dot2_f32_bf16 v135, v109, v50, v135\n"
    "s_waitcnt lgkmcnt(2)\n"
    "v_dot2_f32_bf16 v136, v110, v50, v136\n"
    "s_waitcnt lgkmcnt(1)\n"
    "v_dot2_f32_bf16 v137, v112, v50, v137\n"
    "v_lshlrev_b32 v146, 16, v118\n"
    "v_and_b32 v147, 0xffff0000, v118\n"
    "v_lshlrev_b32 v148, 16, v119\n"
    "v_and_b32 v149, 0xffff0000, v119\n"
    "s_waitcnt lgkmcnt(0)\n"
    "v_lshlrev_b32 v150, 16, v120\n"
    "v_and_b32 v151, 0xffff0000, v120\n"
    "v_lshlrev_b32 v152, 16, v121\n"
    "v_and_b32 v153, 0xffff0000, v121\n"
    "v_pk_mul_f32 v[130:131], v[130:131], v[146:147]\n"
    "v_pk_mul_f32 v[132:133], v[132:133], v[148:149]\n"
    "v_pk_mul_f32 v[134:135], v[134:135], v[150:151]\n"
    "v_pk_mul_f32 v[136:137], v[136:137], v[152:153]\n"
    "s_waitcnt vmcnt(2)\n"
    "v_mad_u64_u32 v[162:163], s[6:7], v168, v82, v[92:93]\n"
    "s_waitcnt vmcnt(1)\n"
    "v_mad_u64_u32 v[164:165], s[6:7], v172, v82, v[92:93]\n"
    "v_mad_u64_u32 v[162:163], s[6:7], v169, v83, v[162:163]\n"
    "v_mad_u64_u32 v[164:165], s[6:7], v173, v83, v[164:165]\n"
    "v_mad_u64_u32 v[162:163], s[6:7], v170, v84, v[162:163]\n"
    "v_mad_u64_u32 v[164:165], s[6:7], v174, v84, v[164:165]\n"
    "v_mad_u64_u32 v[162:163], s[6:7], v171, v85, v[162:163]\n"
    "v_mad_u64_u32 v[164:165], s[6:7], v175, v85, v[164:165]\n"
    "ds_write_b128 v59, v[168:171] offset:0\n"
    "v_add_u32_dpp v166, v162, v162 row_half_mirror row_mask:0xf bank_mask:0x5\n"
    "v_add_u32_dpp v167, v163, v163 row_half_mirror row_mask:0xf bank_mask:0x5\n"
    "s_nop 0\n"
    "v_add_u32_dpp v166, v164, v164 row_half_mirror row_mask:0xf bank_mask:0xa\n"
    "v_add_u32_dpp v167, v165, v165 row_half_mirror row_mask:0xf bank_mask:0xa\n"
    "ds_write_b128 v59, v[172:175] offset:256\n"
    "v_add_u32_dpp v166, v166, v166 quad_perm:[1,0,3,2] row_mask:0xf bank_mask:0xf\n"
    "v_add_u32_dpp v167, v167, v167 quad_perm:[1,0,3,2] row_mask:0xf bank_mask:0xf\n"
    "s_nop 0\n"
    "v_add_u32_dpp v166, v166, v166 quad_perm:[2,3,0,1] row_mask:0xf bank_mask:0xf\n"
    "v_add_u32_dpp v167, v167, v167 quad_perm:[2,3,0,1] row_mask:0xf bank_mask:0xf\n"
    "s_waitcnt vmcnt(0)\n"
    "v_cmp_ne_u64 vcc, v[166:167], v[176:177]\n"
    "s_cbranch_vccnz Lsg1\n"
    "Lsg1_r:\n"
    "ds_write_b8 v63, v68\n"
    "buffer_load_dwordx4 v[10:13], v209, s[20:23], s71 offen sc1\n"
    "buffer_load_dwordx4 v[14:17], v209, s[20:23], s71 offen offset:64 sc1\n"
    "buffer_load_dwordx4 v[18:21], v52, s[20:23], s90 offen sc1\n"
    "buffer_load_dwordx4 v[22:25], v52, s[20:23], s91 offen sc1\n"
    "buffer_load_dwordx4 v[190:193], v4, s[32:35], s65 offen\n"
    "buffer_load_dwordx4 v[194:197], v4, s[32:35], s66 offen\n"
    "v_cvt_pk_bf16_f32 v154, v129, v128\n"
    "v_cvt_pk_bf16_f32 v155, v127, v126\n"
    "v_cvt_pk_bf16_f32 v156, v125, v124\n"
    "v_cvt_pk_bf16_f32 v157, v123, v122\n"
    "v_cvt_pk_bf16_f32 v158, v137, v136\n"
    "v_cvt_pk_bf16_f32 v159, v135, v134\n"
    "v_cvt_pk_bf16_f32 v160, v133, v132\n"
    "v_cvt_pk_bf16_f32 v161, v131, v130\n"
    "v_mad_u64_u32 v[162:163], s[6:7], v154, v74, v[90:91]\n"
    "v_mad_u64_u32 v[164:165], s[6:7], v158, v78, 0\n"
    "v_mad_u64_u32 v[162:163], s[6:7], v155, v75, v[162:163]\n"
    "v_mad_u64_u32 v[164:165], s[6:7], v159, v79, v[164:165]\n"
    "v_mad_u64_u32 v[162:163], s[6:7], v156, v76, v[162:163]\n"
    "v_mad_u64_u32 v[164:165], s[6:7], v160, v80, v[164:165]\n"
    "v_mad_u64_u32 v[162:163], s[6:7], v157, v77, v[162:163]\n"
    "v_mad_u64_u32 v[164:165], s[6:7], v161, v81, v[164:165]\n"
    "v_add_u32 v166, v162, v164\n"
    "v_add_u32 v167, v163, v165\n"
    "v_mov_b32 v138, v154\n"
    "v_mov_b32 v139, v155\n"
    "v_mov_b32 v140, v156\n"
    "v_mov_b32 v141, v157\n"
    "v_add_u32_dpp v166, v166, v166 quad_perm:[1,0,3,2] row_mask:0xf bank_mask:0xf\n"
    "v_add_u32_dpp v167, v167, v167 quad_perm:[1,0,3,2] row_mask:0xf bank_mask:0xf\n"
    "v_mov_b32_dpp v154, v158 row_shr:4 row_mask:0xf bank_mask:0xa\n"
    "v_mov_b32_dpp v155, v159 row_shr:4 row_mask:0xf bank_mask:0xa\n"
    "v_mov_b32_dpp v156, v160 row_shr:4 row_mask:0xf bank_mask:0xa\n"
    "v_mov_b32_dpp v157, v161 row_shr:4 row_mask:0xf bank_mask:0xa\n"
    "v_add_u32_dpp v166, v166, v166 quad_perm:[2,3,0,1] row_mask:0xf bank_mask:0xf\n"
    "v_add_u32_dpp v167, v167, v167 quad_perm:[2,3,0,1] row_mask:0xf bank_mask:0xf\n"
    "buffer_store_dwordx4 v[154:157], v72, s[20:23], s69 offen sc1\n"
    "buffer_store_dwordx2 v[166:167], v56, s[20:23], s69 offen offset:2048 sc1\n"
    "v_mov_b32_dpp v158, v138 row_shl:4 row_mask:0xf bank_mask:0x5\n"
    "v_mov_b32_dpp v159, v139 row_shl:4 row_mask:0xf bank_mask:0x5\n"
    "v_mov_b32_dpp v160, v140 row_shl:4 row_mask:0xf bank_mask:0x5\n"
    "v_mov_b32_dpp v161, v141 row_shl:4 row_mask:0xf bank_mask:0x5\n"
    "buffer_store_dwordx4 v[158:161], v72, s[20:23], s69 offen offset:128 sc1\n"
    "s_waitcnt vmcnt(8)\n"
    "v_bitop3_b32 v8, v10, v12, s58 bitop3:0x96\n"
    "v_bitop3_b32 v9, v11, v13, s59 bitop3:0x96\n"
    "v_or_b32 v71, v8, v9\n"
    "s_waitcnt vmcnt(7)\n"
    "v_bitop3_b32 v8, v14, v16, s58 bitop3:0x96\n"
    "v_bitop3_b32 v9, v15, v17, s59 bitop3:0x96\n"
    "v_or3_b32 v71, v8, v9, v71\n"
    "s_waitcnt vmcnt(6)\n"
    "v_bitop3_b32 v8, v18, v20, s58 bitop3:0x96\n"
    "v_bitop3_b32 v9, v19, v21, s59 bitop3:0x96\n"
    "v_or3_b32 v71, v8, v9, v71\n"
    "s_waitcnt vmcnt(5)\n"
    "v_bitop3_b32 v8, v22, v24, s58 bitop3:0x96\n"
    "v_bitop3_b32 v9, v23, v25, s59 bitop3:0x96\n"
    "v_or3_b32 v71, v8, v9, v71\n"
    "v_cmp_ne_u32 vcc, 0, v71\n"
    "s_cbranch_vccnz Lsm\n"
    "Lsm_r:\n"
    "s_mov_b32 vcc_lo, 0xff00ff00\n"
    "s_mov_b32 vcc_hi, vcc_lo\n"
    "v_cndmask_b32 v10, v10, v11, vcc\n"
    "v_cndmask_b32 v11, v14, v15, vcc\n"
    "v_lshlrev_b32 v12, 16, v10\n"
    "v_and_b32 v10, 0xffff0000, v10\n"
    "v_lshlrev_b32 v13, 16, v11\n"
    "v_and_b32 v11, 0xffff0000, v11\n"
    "v_rcp_f32 v10, v10\n"
    "v_rcp_f32 v11, v11\n"
    "v_mul_f32 v12, -v12, v10\n"
    "v_mul_f32 v13, -v13, v11\n"
    "v_lshlrev_b32 v20, 16, v18\n"
    "v_and_b32 v18, 0xffff0000, v18\n"
    "v_lshlrev_b32 v21, 16, v19\n"
    "v_and_b32 v19, 0xffff0000, v19\n"
    "v_rcp_f32 v18, v18\n"
    "v_rcp_f32 v19, v19\n"
    "v_mul_f32 v20, -v20, v18\n"
    "v_mul_f32 v21, -v21, v19\n"
    "v_lshlrev_b32 v24, 16, v22\n"
    "v_and_b32 v22, 0xffff0000, v22\n"
    "v_lshlrev_b32 v25, 16, v23\n"
    "v_and_b32 v23, 0xffff0000, v23\n"
    "v_rcp_f32 v22, v22\n"
    "v_rcp_f32 v23, v23\n"
    "v_mul_f32 v24, -v24, v22\n"
    "v_mul_f32 v25, -v25, v23\n"
    "s_waitcnt vmcnt(4)\n"
    "v_lshlrev_b32 v228, 16, v190\n"
    "v_fma_f32 v212, v228, v10, v12\n"
    "v_add_f32 v228, v228, v212\n"
    "v_and_b32 v229, 0xffff0000, v190\n"
    "v_fma_f32 v213, v229, v10, v12\n"
    "v_add_f32 v229, v229, v213\n"
    "v_lshlrev_b32 v230, 16, v191\n"
    "v_fma_f32 v214, v230, v10, v12\n"
    "v_add_f32 v230, v230, v214\n"
    "v_and_b32 v231, 0xffff0000, v191\n"
    "v_fma_f32 v215, v231, v10, v12\n"
    "v_add_f32 v231, v231, v215\n"
    "v_lshlrev_b32 v232, 16, v192\n"
    "v_fma_f32 v216, v232, v10, v12\n"
    "v_add_f32 v232, v232, v216\n"
    "v_and_b32 v233, 0xffff0000, v192\n"
    "v_fma_f32 v217, v233, v10, v12\n"
    "v_add_f32 v233, v233, v217\n"
    "v_lshlrev_b32 v234, 16, v193\n"
    "v_fma_f32 v218, v234, v10, v12\n"
    "v_add_f32 v234, v234, v218\n"
    "v_and_b32 v235, 0xffff0000, v193\n"
    "v_fma_f32 v219, v235, v10, v12\n"
    "v_add_f32 v235, v235, v219\n"
    "s_waitcnt vmcnt(3)\n"
    "v_lshlrev_b32 v236, 16, v194\n"
    "v_fma_f32 v220, v236, v11, v13\n"
    "v_add_f32 v236, v236, v220\n"
    "v_and_b32 v237, 0xffff0000, v194\n"
    "v_fma_f32 v221, v237, v11, v13\n"
    "v_add_f32 v237, v237, v221\n"
    "v_lshlrev_b32 v238, 16, v195\n"
    "v_fma_f32 v222, v238, v11, v13\n"
    "v_add_f32 v238, v238, v222\n"
    "v_and_b32 v239, 0xffff0000, v195\n"
    "v_fma_f32 v223, v239, v11, v13\n"
    "v_add_f32 v239, v239, v223\n"
    "v_lshlrev_b32 v240, 16, v196\n"
    "v_fma_f32 v224, v240, v11, v13\n"
    "v_add_f32 v240, v240, v224\n"
    "v_and_b32 v241, 0xffff0000, v196\n"
    "v_fma_f32 v225, v241, v11, v13\n"
    "v_add_f32 v241, v241, v225\n"
    "v_lshlrev_b32 v242, 16, v197\n"
    "v_fma_f32 v226, v242, v11, v13\n"
    "v_add_f32 v242, v242, v226\n"
    "v_and_b32 v243, 0xffff0000, v197\n"
    "v_fma_f32 v227, v243, v11, v13\n"
    "v_add_f32 v243, v243, v227\n"
    "s_waitcnt lgkmcnt(0)\n"
    "s_mov_b32 s4, 4194304\n"
    "Lw2:\n"
    "ds_read_b32 v8, v62 offset:512\n"
    "s_waitcnt lgkmcnt(0)\n"
    "v_readfirstlane_b32 s5, v8\n"
    "s_cmp_eq_u32 s5, 0x01010101\n"
    "s_cbranch_scc1 Lw2x\n"
    "s_sleep 2\n"
    "s_sub_u32 s4, s4, 1\n"
    "s_cbranch_scc0 Lw2\n"
    "Lw2x:\n"
    "buffer_load_dwordx4 v[180:183], v57, s[20:23], s79 offen sc1\n"
    "buffer_load_dwordx4 v[184:187], v57, s[20:23], s80 offen sc1\n"
    "buffer_load_dwordx2 v[188:189], v58, s[20:23], s79 offen offset:2048 sc1\n"
    "s_mov_b32 s4, 4194304\n"
    "Lw3:\n"
    "ds_read_b32 v8, v62 offset:512\n"
    "s_waitcnt lgkmcnt(0)\n"
    "v_readfirstlane_b32 s5, v8\n"
    "s_cmp_eq_u32 s5, 0x01010101\n"
    "s_cbranch_scc1 Lw3x\n"
    "s_sleep 2\n"
    "s_sub_u32 s4, s4, 1\n"
    "s_cbranch_scc0 Lw3\n"
    "Lw3x:\n"
    "ds_read_b64 v[138:139], v206 offset:0\n"
    "ds_read_b64 v[140:141], v206 offset:10240\n"
    "ds_read_b64 v[142:143], v206 offset:20480\n"
    "ds_read_b64 v[144:145], v206 offset:30720\n"
    "ds_read_b64 v[146:147], v206 offset:128\n"
    "ds_read_b64 v[148:149], v206 offset:10368\n"
    "ds_read_b64 v[150:151], v206 offset:20608\n"
    "ds_read_b64 v[152:153], v206 offset:30848\n"
    "s_waitcnt lgkmcnt(6)\n"
    "v_cvt_pk_bf16_f32 v154, v138, v140\n"
    "s_waitcnt lgkmcnt(4)\n"
    "v_cvt_pk_bf16_f32 v155, v142, v144\n"
    "v_cvt_pk_bf16_f32 v156, v139, v141\n"
    "v_cvt_pk_bf16_f32 v157, v143, v145\n"
    "buffer_store_dwordx4 v[154:157], v52, s[20:23], s70 offen offset:0 sc1\n"
    "s_waitcnt lgkmcnt(2)\n"
    "v_cvt_pk_bf16_f32 v158, v146, v148\n"
    "s_waitcnt lgkmcnt(0)\n"
    "v_cvt_pk_bf16_f32 v159, v150, v152\n"
    "v_cvt_pk_bf16_f32 v160, v147, v149\n"
    "v_cvt_pk_bf16_f32 v161, v151, v153\n"
    "buffer_store_dwordx4 v[158:161], v52, s[20:23], s70 offen offset:1024 sc1\n"
    "v_mad_u64_u32 v[162:163], s[6:7], v154, v82, v[92:93]\n"
    "v_mad_u64_u32 v[164:165], s[6:7], v158, v82, v[92:93]\n"
    "v_mad_u64_u32 v[162:163], s[6:7], v155, v83, v[162:163]\n"
    "v_mad_u64_u32 v[164:165], s[6:7], v159, v83, v[164:165]\n"
    "v_mad_u64_u32 v[162:163], s[6:7], v156, v84, v[162:163]\n"
    "v_mad_u64_u32 v[164:165], s[6:7], v160, v84, v[164:165]\n"
    "v_mad_u64_u32 v[162:163], s[6:7], v157, v85, v[162:163]\n"
    "v_mad_u64_u32 v[164:165], s[6:7], v161, v85, v[164:165]\n"
    "s_nop 0\n"
    "v_add_u32_dpp v166, v162, v162 row_half_mirror row_mask:0xf bank_mask:0x5\n"
    "v_add_u32_dpp v167, v163, v163 row_half_mirror row_mask:0xf bank_mask:0x5\n"
    "s_nop 0\n"
    "v_add_u32_dpp v166, v164, v164 row_half_mirror row_mask:0xf bank_mask:0xa\n"
    "v_add_u32_dpp v167, v165, v165 row_half_mirror row_mask:0xf bank_mask:0xa\n"
    "s_nop 0\n"
    "v_add_u32_dpp v166, v166, v166 quad_perm:[1,0,3,2] row_mask:0xf bank_mask:0xf\n"
    "v_add_u32_dpp v167, v167, v167 quad_perm:[1,0,3,2] row_mask:0xf bank_mask:0xf\n"
    "s_nop 0\n"
    "v_add_u32_dpp v166, v166, v166 quad_perm:[2,3,0,1] row_mask:0xf bank_mask:0xf\n"
    "v_add_u32_dpp v167, v167, v167 quad_perm:[2,3,0,1] row_mask:0xf bank_mask:0xf\n"
    "buffer_store_dwordx2 v[166:167], v210, s[20:23], s70 offen offset:2048 sc1\n"
    "buffer_load_dwordx4 v[198:201], v60, s[20:23], s92 offen sc1\n"
    "buffer_load_dwordx4 v[202:205], v60, s[20:23], s93 offen sc1\n"
    "buffer_load_dwordx2 v[28:29], v61, s[20:23], s92 offen offset:2048 sc1\n"
    "s_waitcnt vmcnt(8)\n"
    "v_mad_u64_u32 v[162:163], s[6:7], v180, v82, v[92:93]\n"
    "s_waitcnt vmcnt(7)\n"
    "v_mad_u64_u32 v[164:165], s[6:7], v184, v82, v[92:93]\n"
    "v_mad_u64_u32 v[162:163], s[6:7], v181, v83, v[162:163]\n"
    "v_mad_u64_u32 v[164:165], s[6:7], v185, v83, v[164:165]\n"
    "v_mad_u64_u32 v[162:163], s[6:7], v182, v84, v[162:163]\n"
    "v_mad_u64_u32 v[164:165], s[6:7], v186, v84, v[164:165]\n"
    "v_mad_u64_u32 v[162:163], s[6:7], v183, v85, v[162:163]\n"
    "v_mad_u64_u32 v[164:165], s[6:7], v187, v85, v[164:165]\n"
    "ds_write_b128 v59, v[180:183] offset:2048\n"
    "v_add_u32_dpp v166, v162, v162 row_half_mirror row_mask:0xf bank_mask:0x5\n"
    "v_add_u32_dpp v167, v163, v163 row_half_mirror row_mask:0xf bank_mask:0x5\n"
    "s_nop 0\n"
    "v_add_u32_dpp v166, v164, v164 row_half_mirror row_mask:0xf bank_mask:0xa\n"
    "v_add_u32_dpp v167, v165, v165 row_half_mirror row_mask:0xf bank_mask:0xa\n"
    "ds_write_b128 v59, v[184:187] offset:2304\n"
    "v_add_u32_dpp v166, v166, v166 quad_perm:[1,0,3,2] row_mask:0xf bank_mask:0xf\n"
    "v_add_u32_dpp v167, v167, v167 quad_perm:[1,0,3,2] row_mask:0xf bank_mask:0xf\n"
    "s_nop 0\n"
    "v_add_u32_dpp v166, v166, v166 quad_perm:[2,3,0,1] row_mask:0xf bank_mask:0xf\n"
    "v_add_u32_dpp v167, v167, v167 quad_perm:[2,3,0,1] row_mask:0xf bank_mask:0xf\n"
    "s_waitcnt vmcnt(6)\n"
    "v_cmp_ne_u64 vcc, v[166:167], v[188:189]\n"
    "s_cbranch_vccnz Lsg2\n"
    "Lsg2_r:\n"
    "ds_write_b8 v63, v68 offset:256\n"
    "buffer_load_dwordx4 v[46:49], off, s[20:23], s75 sc1\n"
    "s_waitcnt vmcnt(0)\n"
    "v_bitop3_b32 v8, v46, v48, s58 bitop3:0x96\n"
    "v_bitop3_b32 v9, v47, v49, s59 bitop3:0x96\n"
    "v_or_b32 v71, v8, v9\n"
    "v_cmp_ne_u32 vcc, 0, v71\n"
    "s_cbranch_vccnz Lsd4\n"
    "Lsd4_r:\n"
    "v_readfirstlane_b32 s78, v46\n"
    "v_readfirstlane_b32 s69, v47\n"
    "s_andn2_b32 s78, 2047, s78\n"
    "s_andn2_b32 s69, 2047, s69\n"
    "v_mov_b32 v5, v4\n"
    "s_lshr_b32 s4, s78, 4\n"
    "s_lshr_b32 s5, s69, 4\n"
    "s_cmp_eq_u32 s4, s95\n"
    "s_cselect_b32 s5, s95, s5\n"
    "s_cmp_eq_u32 s5, s95\n"
    "s_cbranch_scc0 Lsk5\n"
    "v_add_u32 v9, 0, v211\n"
    "v_cmp_ne_u32 vcc, s78, v9\n"
    "v_cmp_ne_u32 s[6:7], s69, v9\n"
    "v_bfrev_b32 v8, 1\n"
    "s_and_b64 vcc, vcc, s[6:7]\n"
    "v_cndmask_b32 v4, v8, v4, vcc\n"
    "v_add_u32 v9, 8, v211\n"
    "v_cmp_ne_u32 vcc, s78, v9\n"
    "v_cmp_ne_u32 s[6:7], s69, v9\n"
    "v_bfrev_b32 v8, 1\n"
    "s_and_b64 vcc, vcc, s[6:7]\n"
    "v_cndmask_b32 v5, v8, v5, vcc\n"
    "Lsk5:\n"
    "s_lshl_b32 s4, s62, 3\n"
    "s_mul_i32 s84, s78, 1280\n"
    "s_add_u32 s84, s84, s4\n"
    "s_lshr_b32 s82, s78, 1\n"
    "s_lshl_b32 s82, s82, 4\n"
    "s_add_u32 s82, s82, 61440\n"
    "s_bfe_u32 s76, s78, 0x20007\n"
    "s_cmp_lg_u32 s76, s3\n"
    "s_cbranch_scc1 Lpa6\n"
    "buffer_load_ushort v26, v55, s[32:35], s84 offen\n"
    "buffer_load_dwordx4 v[30:33], off, s[20:23], s82 sc1\n"
    "s_lshr_b32 s5, s78, 8\n"
    "s_mul_i32 s6, s5, 1280\n"
    "s_bfe_u32 s5, s78, 0x40004\n"
    "s_lshl_b32 s5, s5, 4\n"
    "s_add_u32 s6, s6, s5\n"
    "s_bfe_u32 s5, s78, 0x20002\n"
    "s_mul_i32 s5, s5, 320\n"
    "s_add_u32 s6, s6, s5\n"
    "s_and_b32 s5, s78, 3\n"
    "s_lshl_b32 s5, s5, 2\n"
    "s_add_u32 s6, s6, s5\n"
    "s_add_u32 s6, s6, 85120\n"
    "v_mul_u32_u24 v96, 10240, v69\n"
    "v_add_u32 v96, s6, v96\n"
    "Lpa6:\n"
    "s_mul_i32 s85, s69, 1280\n"
    "s_add_u32 s85, s85, s4\n"
    "s_lshr_b32 s83, s69, 1\n"
    "s_lshl_b32 s83, s83, 4\n"
    "s_add_u32 s83, s83, 61440\n"
    "s_bfe_u32 s77, s69, 0x20007\n"
    "s_cmp_lg_u32 s77, s3\n"
    "s_cbranch_scc1 Lpa7\n"
    "buffer_load_ushort v27, v55, s[32:35], s85 offen\n"
    "buffer_load_dwordx4 v[34:37], off, s[20:23], s83 sc1\n"
    "s_lshr_b32 s5, s69, 8\n"
    "s_mul_i32 s6, s5, 1280\n"
    "s_bfe_u32 s5, s69, 0x40004\n"
    "s_lshl_b32 s5, s5, 4\n"
    "s_add_u32 s6, s6, s5\n"
    "s_bfe_u32 s5, s69, 0x20002\n"
    "s_mul_i32 s5, s5, 320\n"
    "s_add_u32 s6, s6, s5\n"
    "s_and_b32 s5, s69, 3\n"
    "s_lshl_b32 s5, s5, 2\n"
    "s_add_u32 s6, s6, s5\n"
    "s_add_u32 s6, s6, 85120\n"
    "v_mul_u32_u24 v97, 10240, v69\n"
    "v_add_u32 v97, s6, v97\n"
    "Lpa7:\n"
    "v_mad_u64_u32 v[162:163], s[6:7], v198, v82, v[92:93]\n"
    "v_mad_u64_u32 v[164:165], s[6:7], v202, v82, v[92:93]\n"
    "v_mad_u64_u32 v[162:163], s[6:7], v199, v83, v[162:163]\n"
    "v_mad_u64_u32 v[164:165], s[6:7], v203, v83, v[164:165]\n"
    "v_mad_u64_u32 v[162:163], s[6:7], v200, v84, v[162:163]\n"
    "v_mad_u64_u32 v[164:165], s[6:7], v204, v84, v[164:165]\n"
    "v_mad_u64_u32 v[162:163], s[6:7], v201, v85, v[162:163]\n"
    "v_mad_u64_u32 v[164:165], s[6:7], v205, v85, v[164:165]\n"
    "s_nop 0\n"
    "v_add_u32_dpp v166, v162, v162 row_half_mirror row_mask:0xf bank_mask:0x5\n"
    "v_add_u32_dpp v167, v163, v163 row_half_mirror row_mask:0xf bank_mask:0x5\n"
    "s_nop 0\n"
    "v_add_u32_dpp v166, v164, v164 row_half_mirror row_mask:0xf bank_mask:0xa\n"
    "v_add_u32_dpp v167, v165, v165 row_half_mirror row_mask:0xf bank_mask:0xa\n"
    "s_nop 0\n"
    "v_add_u32_dpp v166, v166, v166 quad_perm:[1,0,3,2] row_mask:0xf bank_mask:0xf\n"
    "v_add_u32_dpp v167, v167, v167 quad_perm:[1,0,3,2] row_mask:0xf bank_mask:0xf\n"
    "s_nop 0\n"
    "v_add_u32_dpp v166, v166, v166 quad_perm:[2,3,0,1] row_mask:0xf bank_mask:0xf\n"
    "v_add_u32_dpp v167, v167, v167 quad_perm:[2,3,0,1] row_mask:0xf bank_mask:0xf\n"
    "v_cmp_ne_u64 vcc, v[166:167], v[28:29]\n"
    "s_cbranch_vccnz Lsx8\n"
    "Lsx8_r:\n"
    "v_lshlrev_b32 v122, 16, v198\n"
    "v_and_b32 v123, 0xffff0000, v198\n"
    "v_lshlrev_b32 v124, 16, v199\n"
    "v_and_b32 v125, 0xffff0000, v199\n"
    "v_lshlrev_b32 v126, 16, v200\n"
    "v_and_b32 v127, 0xffff0000, v200\n"
    "v_lshlrev_b32 v128, 16, v201\n"
    "v_and_b32 v129, 0xffff0000, v201\n"
    "v_fma_f32 v130, v122, v18, v20\n"
    "v_fma_f32 v131, v123, v18, v20\n"
    "v_fma_f32 v132, v124, v18, v20\n"
    "v_fma_f32 v133, v125, v18, v20\n"
    "v_fma_f32 v134, v126, v19, v21\n"
    "v_fma_f32 v135, v127, v19, v21\n"
    "v_fma_f32 v136, v128, v19, v21\n"
    "v_fma_f32 v137, v129, v19, v21\n"
    "buffer_load_dwordx4 v[168:171], v57, s[20:23], s67 offen sc1\n"
    "buffer_load_dwordx4 v[172:175], v57, s[20:23], s81 offen sc1\n"
    "buffer_load_dwordx2 v[176:177], v58, s[20:23], s67 offen offset:2048 sc1\n"
    "v_lshlrev_b32 v74, 16, v202\n"
    "v_and_b32 v75, 0xffff0000, v202\n"
    "v_lshlrev_b32 v76, 16, v203\n"
    "v_and_b32 v77, 0xffff0000, v203\n"
    "v_lshlrev_b32 v78, 16, v204\n"
    "v_and_b32 v79, 0xffff0000, v204\n"
    "v_lshlrev_b32 v80, 16, v205\n"
    "v_and_b32 v81, 0xffff0000, v205\n"
    "v_fma_f32 v106, v74, v22, v24\n"
    "v_fma_f32 v107, v75, v22, v24\n"
    "v_fma_f32 v108, v76, v22, v24\n"
    "v_fma_f32 v109, v77, v22, v24\n"
    "v_fma_f32 v110, v78, v23, v25\n"
    "v_fma_f32 v111, v79, v23, v25\n"
    "v_fma_f32 v112, v80, v23, v25\n"
    "v_fma_f32 v113, v81, v23, v25\n"
    "s_waitcnt lgkmcnt(0)\n"
    "s_mov_b32 s4, 4194304\n"
    "Lw10:\n"
    "ds_read_b32 v8, v62 offset:768\n"
    "s_waitcnt lgkmcnt(0)\n"
    "v_readfirstlane_b32 s5, v8\n"
    "s_cmp_eq_u32 s5, 0x01010101\n"
    "s_cbranch_scc1 Lw10x\n"
    "s_sleep 2\n"
    "s_sub_u32 s4, s4, 1\n"
    "s_cbranch_scc0 Lw10\n"
    "Lw10x:\n"
    "ds_read_b64 v[114:115], v64 offset:5120\n"
    "ds_read_b64 v[116:117], v64 offset:15360\n"
    "ds_read_b64 v[118:119], v64 offset:25600\n"
    "ds_read_b64 v[120:121], v64 offset:35840\n"
    "s_waitcnt lgkmcnt(3)\n"
    "v_fma_f32 v122, v130, v114, v122\n"
    "s_waitcnt lgkmcnt(2)\n"
    "v_fma_f32 v123, v131, v116, v123\n"
    "s_waitcnt lgkmcnt(1)\n"
    "v_fma_f32 v124, v132, v118, v124\n"
    "s_waitcnt lgkmcnt(0)\n"
    "v_fma_f32 v125, v133, v120, v125\n"
    "v_cvt_pk_bf16_f32 v154, v122, v123\n"
    "v_cvt_pk_bf16_f32 v155, v124, v125\n"
    "buffer_store_dwordx2 v[154:155], v65, s[36:39], s74 offen offset:0\n"
    "v_fma_f32 v126, v134, v115, v126\n"
    "v_fma_f32 v127, v135, v117, v127\n"
    "v_fma_f32 v128, v136, v119, v128\n"
    "v_fma_f32 v129, v137, v121, v129\n"
    "v_cvt_pk_bf16_f32 v156, v126, v127\n"
    "v_cvt_pk_bf16_f32 v157, v128, v129\n"
    "buffer_store_dwordx2 v[156:157], v65, s[36:39], s74 offen offset:1280\n"
    "s_waitcnt vmcnt(4)\n"
    "v_mad_u64_u32 v[162:163], s[6:7], v168, v82, v[92:93]\n"
    "s_waitcnt vmcnt(3)\n"
    "v_mad_u64_u32 v[164:165], s[6:7], v172, v82, v[92:93]\n"
    "v_mad_u64_u32 v[162:163], s[6:7], v169, v83, v[162:163]\n"
    "v_mad_u64_u32 v[164:165], s[6:7], v173, v83, v[164:165]\n"
    "v_mad_u64_u32 v[162:163], s[6:7], v170, v84, v[162:163]\n"
    "v_mad_u64_u32 v[164:165], s[6:7], v174, v84, v[164:165]\n"
    "v_mad_u64_u32 v[162:163], s[6:7], v171, v85, v[162:163]\n"
    "v_mad_u64_u32 v[164:165], s[6:7], v175, v85, v[164:165]\n"
    "ds_write2_b64 v207, v[168:169], v[170:171] offset0:0 offset1:146\n"
    "v_add_u32_dpp v166, v162, v162 row_half_mirror row_mask:0xf bank_mask:0x5\n"
    "v_add_u32_dpp v167, v163, v163 row_half_mirror row_mask:0xf bank_mask:0x5\n"
    "s_nop 0\n"
    "v_add_u32_dpp v166, v164, v164 row_half_mirror row_mask:0xf bank_mask:0xa\n"
    "v_add_u32_dpp v167, v165, v165 row_half_mirror row_mask:0xf bank_mask:0xa\n"
    "ds_write2_b64 v207, v[172:173], v[174:175] offset0:8 offset1:154\n"
    "v_add_u32_dpp v166, v166, v166 quad_perm:[1,0,3,2] row_mask:0xf bank_mask:0xf\n"
    "v_add_u32_dpp v167, v167, v167 quad_perm:[1,0,3,2] row_mask:0xf bank_mask:0xf\n"
    "ds_read_b128 v[180:183], v208\n"
    "ds_read_b128 v[184:187], v208 offset:576\n"
    "v_add_u32_dpp v166, v166, v166 quad_perm:[2,3,0,1] row_mask:0xf bank_mask:0xf\n"
    "v_add_u32_dpp v167, v167, v167 quad_perm:[2,3,0,1] row_mask:0xf bank_mask:0xf\n"
    "s_waitcnt vmcnt(2)\n"
    "v_cmp_ne_u64 vcc, v[166:167], v[176:177]\n"
    "s_cbranch_vccnz Lsgy11\n"
    "Lsgy11_r:\n"
    "s_waitcnt lgkmcnt(1)\n"
    "v_lshlrev_b32 v9, 16, v180\n"
    "v_fmac_f32 v228, v212, v9\n"
    "v_and_b32 v9, 0xffff0000, v180\n"
    "v_fmac_f32 v229, v213, v9\n"
    "v_cvt_pk_bf16_f32 v154, v228, v229\n"
    "v_lshlrev_b32 v9, 16, v181\n"
    "v_fmac_f32 v230, v214, v9\n"
    "v_and_b32 v9, 0xffff0000, v181\n"
    "v_fmac_f32 v231, v215, v9\n"
    "v_cvt_pk_bf16_f32 v155, v230, v231\n"
    "v_lshlrev_b32 v9, 16, v182\n"
    "v_fmac_f32 v232, v216, v9\n"
    "v_and_b32 v9, 0xffff0000, v182\n"
    "v_fmac_f32 v233, v217, v9\n"
    "v_cvt_pk_bf16_f32 v156, v232, v233\n"
    "v_lshlrev_b32 v9, 16, v183\n"
    "v_fmac_f32 v234, v218, v9\n"
    "v_and_b32 v9, 0xffff0000, v183\n"
    "v_fmac_f32 v235, v219, v9\n"
    "v_cvt_pk_bf16_f32 v157, v234, v235\n"
    "buffer_store_dwordx4 v[154:157], v4, s[36:39], s65 offen\n"
    "s_waitcnt lgkmcnt(0)\n"
    "v_lshlrev_b32 v9, 16, v184\n"
    "v_fmac_f32 v236, v220, v9\n"
    "v_and_b32 v9, 0xffff0000, v184\n"
    "v_fmac_f32 v237, v221, v9\n"
    "v_cvt_pk_bf16_f32 v158, v236, v237\n"
    "v_lshlrev_b32 v9, 16, v185\n"
    "v_fmac_f32 v238, v222, v9\n"
    "v_and_b32 v9, 0xffff0000, v185\n"
    "v_fmac_f32 v239, v223, v9\n"
    "v_cvt_pk_bf16_f32 v159, v238, v239\n"
    "v_lshlrev_b32 v9, 16, v186\n"
    "v_fmac_f32 v240, v224, v9\n"
    "v_and_b32 v9, 0xffff0000, v186\n"
    "v_fmac_f32 v241, v225, v9\n"
    "v_cvt_pk_bf16_f32 v160, v240, v241\n"
    "v_lshlrev_b32 v9, 16, v187\n"
    "v_fmac_f32 v242, v226, v9\n"
    "v_and_b32 v9, 0xffff0000, v187\n"
    "v_fmac_f32 v243, v227, v9\n"
    "v_cvt_pk_bf16_f32 v161, v242, v243\n"
    "buffer_store_dwordx4 v[158:161], v5, s[36:39], s66 offen\n"
    "s_cmp_lg_u32 s76, s3\n"
    "s_cbranch_scc1 Lnp12\n"
    "buffer_load_dwordx4 v[38:41], v66, s[20:23], s73 offen offset:0 sc1\n"
    "v_bitop3_b32 v8, v30, v32, s58 bitop3:0x96\n"
    "v_bitop3_b32 v9, v31, v33, s59 bitop3:0x96\n"
    "v_or_b32 v71, v8, v9\n"
    "v_cmp_ne_u32 vcc, 0, v71\n"
    "s_cbranch_vccnz Lspm13\n"
    "Lspm13_r:\n"
    "s_bitcmp1_b32 s78, 0\n"
    "s_cselect_b64 vcc, -1, 0\n"
    "v_lshlrev_b32 v26, 16, v26\n"
    "v_cndmask_b32 v30, v30, v31, vcc\n"
    "s_cmp_lt_u32 s78, 1024\n"
    "s_cselect_b32 s5, 1.0, 0\n"
    "v_lshlrev_b32 v31, 16, v30\n"
    "v_and_b32 v30, 0xffff0000, v30\n"
    "v_rcp_f32 v30, v30\n"
    "v_sub_f32 v31, v26, v31\n"
    "v_mul_f32 v30, v30, v31\n"
    "v_fma_f32 v26, v30, s5, v26\n"
    "Lnp12:\n"
    "s_cmp_lg_u32 s77, s3\n"
    "s_cbranch_scc1 Lnp14\n"
    "buffer_load_dwordx4 v[42:45], v66, s[20:23], s73 offen offset:256 sc1\n"
    "v_bitop3_b32 v8, v34, v36, s58 bitop3:0x96\n"
    "v_bitop3_b32 v9, v35, v37, s59 bitop3:0x96\n"
    "v_or_b32 v71, v8, v9\n"
    "v_cmp_ne_u32 vcc, 0, v71\n"
    "s_cbranch_vccnz Lspm15\n"
    "Lspm15_r:\n"
    "s_bitcmp1_b32 s69, 0\n"
    "s_cselect_b64 vcc, -1, 0\n"
    "v_lshlrev_b32 v27, 16, v27\n"
    "v_cndmask_b32 v34, v34, v35, vcc\n"
    "s_cmp_lt_u32 s69, 1024\n"
    "s_cselect_b32 s5, 1.0, 0\n"
    "v_lshlrev_b32 v35, 16, v34\n"
    "v_and_b32 v34, 0xffff0000, v34\n"
    "v_rcp_f32 v34, v34\n"
    "v_sub_f32 v35, v27, v35\n"
    "v_mul_f32 v34, v34, v35\n"
    "v_fma_f32 v27, v34, s5, v27\n"
    "Lnp14:\n"
    "s_mov_b32 s4, 4194304\n"
    "Lz16:\n"
    "ds_read_b32 v8, v62 offset:1024\n"
    "ds_read_b64 v[114:115], v64 offset:7680\n"
    "ds_read_b64 v[116:117], v64 offset:17920\n"
    "ds_read_b64 v[118:119], v64 offset:28160\n"
    "ds_read_b64 v[120:121], v64 offset:38400\n"
    "s_waitcnt lgkmcnt(4)\n"
    "v_readfirstlane_b32 s5, v8\n"
    "s_cmp_eq_u32 s5, 0x01010101\n"
    "s_cbranch_scc1 Lz16x\n"
    "s_waitcnt lgkmcnt(0)\n"
    "s_sleep 2\n"
    "s_sub_u32 s4, s4, 1\n"
    "s_cbranch_scc0 Lz16\n"
    "Lz16x:\n"
    "s_waitcnt lgkmcnt(3)\n"
    "v_fma_f32 v74, v106, v114, v74\n"
    "s_waitcnt lgkmcnt(2)\n"
    "v_fma_f32 v75, v107, v116, v75\n"
    "s_waitcnt lgkmcnt(1)\n"
    "v_fma_f32 v76, v108, v118, v76\n"
    "s_waitcnt lgkmcnt(0)\n"
    "v_fma_f32 v77, v109, v120, v77\n"
    "v_cvt_pk_bf16_f32 v154, v74, v75\n"
    "v_cvt_pk_bf16_f32 v155, v76, v77\n"
    "buffer_store_dwordx2 v[154:155], v65, s[36:39], s64 offen offset:0\n"
    "v_fma_f32 v78, v110, v115, v78\n"
    "v_fma_f32 v79, v111, v117, v79\n"
    "v_fma_f32 v80, v112, v119, v80\n"
    "v_fma_f32 v81, v113, v121, v81\n"
    "v_cvt_pk_bf16_f32 v156, v78, v79\n"
    "v_cvt_pk_bf16_f32 v157, v80, v81\n"
    "buffer_store_dwordx2 v[156:157], v65, s[36:39], s64 offen offset:1280\n"
    "s_waitcnt vmcnt(2)\n"
    "s_cmp_lg_u32 s76, s3\n"
    "s_cbranch_scc1 Lnq0\n"
    "ds_read_b32 v98, v96\n"
    "v_bitop3_b32 v8, v38, v40, s58 bitop3:0x96\n"
    "v_bitop3_b32 v9, v39, v41, s59 bitop3:0x96\n"
    "v_or_b32 v71, v8, v9\n"
    "v_cmp_ne_u32 vcc, 0, v71\n"
    "s_and_b32 vcc_hi, vcc_hi, 0x3fffff\n"
    "s_or_b32 s5, vcc_lo, vcc_hi\n"
    "s_cbranch_scc1 Lsp0\n"
    "Lsp0_r:\n"
    "s_mov_b32 vcc_lo, 0xcccccccc\n"
    "s_mov_b32 vcc_hi, vcc_lo\n"
    "s_mov_b32 s6, 0xaaaaaaaa\n"
    "s_mov_b32 s7, s6\n"
    "v_cndmask_b32 v139, v39, v38, vcc\n"
    "v_cndmask_b32 v138, v38, v39, vcc\n"
    "v_lshlrev_b32 v8, 16, v139\n"
    "v_and_b32 v139, 0xffff0000, v139\n"
    "v_lshlrev_b32 v140, 16, v138\n"
    "v_and_b32 v141, 0xffff0000, v138\n"
    "s_nop 0\n"
    "v_add_f32_dpp v140, v8, v140 quad_perm:[2,3,0,1] row_mask:0xf bank_mask:0xf\n"
    "v_add_f32_dpp v141, v139, v141 quad_perm:[2,3,0,1] row_mask:0xf bank_mask:0xf\n"
    "v_cndmask_b32 v8, v141, v140, s[6:7]\n"
    "v_cndmask_b32 v142, v140, v141, s[6:7]\n"
    "s_waitcnt lgkmcnt(0)\n"
    "v_fma_f32 v26, v30, v98, v26\n"
    "v_add_f32_dpp v142, v8, v142 quad_perm:[1,0,3,2] row_mask:0xf bank_mask:0xf\n"
    "s_nop 1\n"
    "v_add_f32_dpp v142, v142, v142 row_ror:4 row_mask:0xf bank_mask:0xf\n"
    "s_nop 1\n"
    "v_add_f32_dpp v142, v142, v142 row_ror:8 row_mask:0xf bank_mask:0xf\n"
    "v_mov_b32 v8, v142\n"
    "s_nop 1\n"
    "v_permlane16_swap_b32 v8, v142\n"
    "v_add_f32 v142, v142, v8\n"
    "v_mov_b32 v8, v142\n"
    "s_nop 1\n"
    "v_permlane32_swap_b32 v8, v142\n"
    "v_add_f32 v26, v26, v8\n"
    "v_add_f32 v142, v142, v26\n"
    "v_cvt_pk_bf16_f32 v142, v142, v142\n"
    "buffer_store_short v142, v55, s[36:39], s84 offen\n"
    "s_nop 1\n"
    "Lnq0:\n"
    "s_cmp_lg_u32 s77, s3\n"
    "s_cbranch_scc1 Lnq1\n"
    "ds_read_b32 v98, v97\n"
    "v_bitop3_b32 v8, v42, v44, s58 bitop3:0x96\n"
    "v_bitop3_b32 v9, v43, v45, s59 bitop3:0x96\n"
    "v_or_b32 v71, v8, v9\n"
    "v_cmp_ne_u32 vcc, 0, v71\n"
    "s_and_b32 vcc_hi, vcc_hi, 0x3fffff\n"
    "s_or_b32 s5, vcc_lo, vcc_hi\n"
    "s_cbranch_scc1 Lsp1\n"
    "Lsp1_r:\n"
    "s_mov_b32 vcc_lo, 0xcccccccc\n"
    "s_mov_b32 vcc_hi, vcc_lo\n"
    "s_mov_b32 s6, 0xaaaaaaaa\n"
    "s_mov_b32 s7, s6\n"
    "v_cndmask_b32 v139, v43, v42, vcc\n"
    "v_cndmask_b32 v138, v42, v43, vcc\n"
    "v_lshlrev_b32 v8, 16, v139\n"
    "v_and_b32 v139, 0xffff0000, v139\n"
    "v_lshlrev_b32 v140, 16, v138\n"
    "v_and_b32 v141, 0xffff0000, v138\n"
    "s_nop 0\n"
    "v_add_f32_dpp v140, v8, v140 quad_perm:[2,3,0,1] row_mask:0xf bank_mask:0xf\n"
    "v_add_f32_dpp v141, v139, v141 quad_perm:[2,3,0,1] row_mask:0xf bank_mask:0xf\n"
    "v_cndmask_b32 v8, v141, v140, s[6:7]\n"
    "v_cndmask_b32 v142, v140, v141, s[6:7]\n"
    "s_waitcnt lgkmcnt(0)\n"
    "v_fma_f32 v27, v34, v98, v27\n"
    "v_add_f32_dpp v142, v8, v142 quad_perm:[1,0,3,2] row_mask:0xf bank_mask:0xf\n"
    "s_nop 1\n"
    "v_add_f32_dpp v142, v142, v142 row_ror:4 row_mask:0xf bank_mask:0xf\n"
    "s_nop 1\n"
    "v_add_f32_dpp v142, v142, v142 row_ror:8 row_mask:0xf bank_mask:0xf\n"
    "v_mov_b32 v8, v142\n"
    "s_nop 1\n"
    "v_permlane16_swap_b32 v8, v142\n"
    "v_add_f32 v142, v142, v8\n"
    "v_mov_b32 v8, v142\n"
    "s_nop 1\n"
    "v_permlane32_swap_b32 v8, v142\n"
    "v_add_f32 v27, v27, v8\n"
    "v_add_f32 v142, v142, v27\n"
    "v_cvt_pk_bf16_f32 v142, v142, v142\n"
    "buffer_store_short v142, v55, s[36:39], s85 offen\n"
    "s_nop 1\n"
    "Lnq1:\n"
    "s_endpgm\n"
    "Lsg1:\n"
    "s_waitcnt lgkmcnt(0)\n"
    "s_nop 7\n"
    "s_mov_b32 s4, 262144\n"
    "Lr17:\n"
    "buffer_load_dwordx4 v[168:171], v57, s[20:23], s68 offen sc1\n"
    "buffer_load_dwordx4 v[172:175], v57, s[20:23], s78 offen sc1\n"
    "buffer_load_dwordx2 v[176:177], v58, s[20:23], s68 offen offset:2048 sc1\n"
    "s_waitcnt vmcnt(2)\n"
    "v_mad_u64_u32 v[162:163], s[6:7], v168, v82, v[92:93]\n"
    "s_waitcnt vmcnt(1)\n"
    "v_mad_u64_u32 v[164:165], s[6:7], v172, v82, v[92:93]\n"
    "v_mad_u64_u32 v[162:163], s[6:7], v169, v83, v[162:163]\n"
    "v_mad_u64_u32 v[164:165], s[6:7], v173, v83, v[164:165]\n"
    "v_mad_u64_u32 v[162:163], s[6:7], v170, v84, v[162:163]\n"
    "v_mad_u64_u32 v[164:165], s[6:7], v174, v84, v[164:165]\n"
    "v_mad_u64_u32 v[162:163], s[6:7], v171, v85, v[162:163]\n"
    "v_mad_u64_u32 v[164:165], s[6:7], v175, v85, v[164:165]\n"
    "ds_write_b128 v59, v[168:171] offset:0\n"
    "v_add_u32_dpp v166, v162, v162 row_half_mirror row_mask:0xf bank_mask:0x5\n"
    "v_add_u32_dpp v167, v163, v163 row_half_mirror row_mask:0xf bank_mask:0x5\n"
    "s_nop 0\n"
    "v_add_u32_dpp v166, v164, v164 row_half_mirror row_mask:0xf bank_mask:0xa\n"
    "v_add_u32_dpp v167, v165, v165 row_half_mirror row_mask:0xf bank_mask:0xa\n"
    "ds_write_b128 v59, v[172:175] offset:256\n"
    "v_add_u32_dpp v166, v166, v166 quad_perm:[1,0,3,2] row_mask:0xf bank_mask:0xf\n"
    "v_add_u32_dpp v167, v167, v167 quad_perm:[1,0,3,2] row_mask:0xf bank_mask:0xf\n"
    "s_nop 0\n"
    "v_add_u32_dpp v166, v166, v166 quad_perm:[2,3,0,1] row_mask:0xf bank_mask:0xf\n"
    "v_add_u32_dpp v167, v167, v167 quad_perm:[2,3,0,1] row_mask:0xf bank_mask:0xf\n"
    "s_waitcnt vmcnt(0)\n"
    "v_cmp_ne_u64 vcc, v[166:167], v[176:177]\n"
    "s_cbranch_vccz Lr17x\n"
    "s_waitcnt vmcnt(0) lgkmcnt(0)\n"
    "s_sub_u32 s4, s4, 1\n"
    "s_cbranch_scc0 Lr17\n"
    "Lr17x:\n"
    "s_waitcnt vmcnt(0) lgkmcnt(0)\n"
    "s_nop 7\n"
    "s_branch Lsg1_r\n"
    "Lsm:\n"
    "s_waitcnt vmcnt(0) lgkmcnt(0)\n"
    "s_nop 7\n"
    "s_mov_b32 s4, 262144\n"
    "Lr18:\n"
    "s_sleep 12\n"
    "buffer_load_dwordx4 v[10:13], v209, s[20:23], s71 offen sc1\n"
    "buffer_load_dwordx4 v[14:17], v209, s[20:23], s71 offen offset:64 sc1\n"
    "buffer_load_dwordx4 v[18:21], v52, s[20:23], s90 offen sc1\n"
    "buffer_load_dwordx4 v[22:25], v52, s[20:23], s91 offen sc1\n"
    "s_waitcnt vmcnt(3)\n"
    "v_bitop3_b32 v8, v10, v12, s58 bitop3:0x96\n"
    "v_bitop3_b32 v9, v11, v13, s59 bitop3:0x96\n"
    "v_or_b32 v71, v8, v9\n"
    "s_waitcnt vmcnt(2)\n"
    "v_bitop3_b32 v8, v14, v16, s58 bitop3:0x96\n"
    "v_bitop3_b32 v9, v15, v17, s59 bitop3:0x96\n"
    "v_or3_b32 v71, v8, v9, v71\n"
    "s_waitcnt vmcnt(1)\n"
    "v_bitop3_b32 v8, v18, v20, s58 bitop3:0x96\n"
    "v_bitop3_b32 v9, v19, v21, s59 bitop3:0x96\n"
    "v_or3_b32 v71, v8, v9, v71\n"
    "s_waitcnt vmcnt(0)\n"
    "v_bitop3_b32 v8, v22, v24, s58 bitop3:0x96\n"
    "v_bitop3_b32 v9, v23, v25, s59 bitop3:0x96\n"
    "v_or3_b32 v71, v8, v9, v71\n"
    "v_cmp_ne_u32 vcc, 0, v71\n"
    "s_cbranch_vccz Lr18x\n"
    "s_waitcnt vmcnt(0) lgkmcnt(0)\n"
    "s_sub_u32 s4, s4, 1\n"
    "s_cbranch_scc0 Lr18\n"
    "Lr18x:\n"
    "s_waitcnt vmcnt(0) lgkmcnt(0)\n"
    "s_nop 7\n"
    "s_branch Lsm_r\n"
    "Lsg2:\n"
    "s_waitcnt lgkmcnt(0)\n"
    "s_nop 7\n"
    "s_mov_b32 s4, 262144\n"
    "Lr19:\n"
    "buffer_load_dwordx4 v[180:183], v57, s[20:23], s79 offen sc1\n"
    "buffer_load_dwordx4 v[184:187], v57, s[20:23], s80 offen sc1\n"
    "buffer_load_dwordx2 v[188:189], v58, s[20:23], s79 offen offset:2048 sc1\n"
    "s_waitcnt vmcnt(2)\n"
    "v_mad_u64_u32 v[162:163], s[6:7], v180, v82, v[92:93]\n"
    "s_waitcnt vmcnt(1)\n"
    "v_mad_u64_u32 v[164:165], s[6:7], v184, v82, v[92:93]\n"
    "v_mad_u64_u32 v[162:163], s[6:7], v181, v83, v[162:163]\n"
    "v_mad_u64_u32 v[164:165], s[6:7], v185, v83, v[164:165]\n"
    "v_mad_u64_u32 v[162:163], s[6:7], v182, v84, v[162:163]\n"
    "v_mad_u64_u32 v[164:165], s[6:7], v186, v84, v[164:165]\n"
    "v_mad_u64_u32 v[162:163], s[6:7], v183, v85, v[162:163]\n"
    "v_mad_u64_u32 v[164:165], s[6:7], v187, v85, v[164:165]\n"
    "ds_write_b128 v59, v[180:183] offset:2048\n"
    "v_add_u32_dpp v166, v162, v162 row_half_mirror row_mask:0xf bank_mask:0x5\n"
    "v_add_u32_dpp v167, v163, v163 row_half_mirror row_mask:0xf bank_mask:0x5\n"
    "s_nop 0\n"
    "v_add_u32_dpp v166, v164, v164 row_half_mirror row_mask:0xf bank_mask:0xa\n"
    "v_add_u32_dpp v167, v165, v165 row_half_mirror row_mask:0xf bank_mask:0xa\n"
    "ds_write_b128 v59, v[184:187] offset:2304\n"
    "v_add_u32_dpp v166, v166, v166 quad_perm:[1,0,3,2] row_mask:0xf bank_mask:0xf\n"
    "v_add_u32_dpp v167, v167, v167 quad_perm:[1,0,3,2] row_mask:0xf bank_mask:0xf\n"
    "s_nop 0\n"
    "v_add_u32_dpp v166, v166, v166 quad_perm:[2,3,0,1] row_mask:0xf bank_mask:0xf\n"
    "v_add_u32_dpp v167, v167, v167 quad_perm:[2,3,0,1] row_mask:0xf bank_mask:0xf\n"
    "s_waitcnt vmcnt(0)\n"
    "v_cmp_ne_u64 vcc, v[166:167], v[188:189]\n"
    "s_cbranch_vccz Lr19x\n"
    "s_waitcnt vmcnt(0) lgkmcnt(0)\n"
    "s_sub_u32 s4, s4, 1\n"
    "s_cbranch_scc0 Lr19\n"
    "Lr19x:\n"
    "s_waitcnt vmcnt(0) lgkmcnt(0)\n"
    "s_nop 7\n"
    "s_branch Lsg2_r\n"
    "Lsd4:\n"
    "s_waitcnt lgkmcnt(0)\n"
    "s_nop 7\n"
    "s_mov_b32 s4, 262144\n"
    "Lr20:\n"
    "buffer_load_dwordx4 v[46:49], off, s[20:23], s75 sc1\n"
    "s_waitcnt vmcnt(0)\n"
    "v_bitop3_b32 v8, v46, v48, s58 bitop3:0x96\n"
    "v_bitop3_b32 v9, v47, v49, s59 bitop3:0x96\n"
    "v_or_b32 v71, v8, v9\n"
    "v_cmp_ne_u32 vcc, 0, v71\n"
    "s_cbranch_vccz Lr20x\n"
    "s_waitcnt vmcnt(0) lgkmcnt(0)\n"
    "s_sub_u32 s4, s4, 1\n"
    "s_cbranch_scc0 Lr20\n"
    "Lr20x:\n"
    "s_waitcnt vmcnt(0) lgkmcnt(0)\n"
    "s_nop 7\n"
    "s_branch Lsd4_r\n"
    "Lsx8:\n"
    "s_waitcnt vmcnt(0) lgkmcnt(0)\n"
    "s_nop 7\n"
    "s_mov_b32 s4, 262144\n"
    "Lr21:\n"
    "s_sleep 12\n"
    "buffer_load_dwordx4 v[198:201], v60, s[20:23], s92 offen sc1\n"
    "buffer_load_dwordx4 v[202:205], v60, s[20:23], s93 offen sc1\n"
    "buffer_load_dwordx2 v[28:29], v61, s[20:23], s92 offen offset:2048 sc1\n"
    "s_waitcnt vmcnt(2)\n"
    "v_mad_u64_u32 v[162:163], s[6:7], v198, v82, v[92:93]\n"
    "s_waitcnt vmcnt(1)\n"
    "v_mad_u64_u32 v[164:165], s[6:7], v202, v82, v[92:93]\n"
    "v_mad_u64_u32 v[162:163], s[6:7], v199, v83, v[162:163]\n"
    "v_mad_u64_u32 v[164:165], s[6:7], v203, v83, v[164:165]\n"
    "v_mad_u64_u32 v[162:163], s[6:7], v200, v84, v[162:163]\n"
    "v_mad_u64_u32 v[164:165], s[6:7], v204, v84, v[164:165]\n"
    "v_mad_u64_u32 v[162:163], s[6:7], v201, v85, v[162:163]\n"
    "v_mad_u64_u32 v[164:165], s[6:7], v205, v85, v[164:165]\n"
    "s_nop 0\n"
    "v_add_u32_dpp v166, v162, v162 row_half_mirror row_mask:0xf bank_mask:0x5\n"
    "v_add_u32_dpp v167, v163, v163 row_half_mirror row_mask:0xf bank_mask:0x5\n"
    "s_nop 0\n"
    "v_add_u32_dpp v166, v164, v164 row_half_mirror row_mask:0xf bank_mask:0xa\n"
    "v_add_u32_dpp v167, v165, v165 row_half_mirror row_mask:0xf bank_mask:0xa\n"
    "s_nop 0\n"
    "v_add_u32_dpp v166, v166, v166 quad_perm:[1,0,3,2] row_mask:0xf bank_mask:0xf\n"
    "v_add_u32_dpp v167, v167, v167 quad_perm:[1,0,3,2] row_mask:0xf bank_mask:0xf\n"
    "s_nop 0\n"
    "v_add_u32_dpp v166, v166, v166 quad_perm:[2,3,0,1] row_mask:0xf bank_mask:0xf\n"
    "v_add_u32_dpp v167, v167, v167 quad_perm:[2,3,0,1] row_mask:0xf bank_mask:0xf\n"
    "s_waitcnt vmcnt(0)\n"
    "v_cmp_ne_u64 vcc, v[166:167], v[28:29]\n"
    "s_cbranch_vccz Lr21x\n"
    "s_waitcnt vmcnt(0) lgkmcnt(0)\n"
    "s_sub_u32 s4, s4, 1\n"
    "s_cbranch_scc0 Lr21\n"
    "Lr21x:\n"
    "s_waitcnt vmcnt(0) lgkmcnt(0)\n"
    "s_nop 7\n"
    "s_branch Lsx8_r\n"
    "Lsgy11:\n"
    "s_waitcnt lgkmcnt(0)\n"
    "s_nop 7\n"
    "s_mov_b32 s4, 262144\n"
    "Lr22:\n"
    "buffer_load_dwordx4 v[168:171], v57, s[20:23], s67 offen sc1\n"
    "buffer_load_dwordx4 v[172:175], v57, s[20:23], s81 offen sc1\n"
    "buffer_load_dwordx2 v[176:177], v58, s[20:23], s67 offen offset:2048 sc1\n"
    "s_waitcnt vmcnt(2)\n"
    "v_mad_u64_u32 v[162:163], s[6:7], v168, v82, v[92:93]\n"
    "s_waitcnt vmcnt(1)\n"
    "v_mad_u64_u32 v[164:165], s[6:7], v172, v82, v[92:93]\n"
    "v_mad_u64_u32 v[162:163], s[6:7], v169, v83, v[162:163]\n"
    "v_mad_u64_u32 v[164:165], s[6:7], v173, v83, v[164:165]\n"
    "v_mad_u64_u32 v[162:163], s[6:7], v170, v84, v[162:163]\n"
    "v_mad_u64_u32 v[164:165], s[6:7], v174, v84, v[164:165]\n"
    "v_mad_u64_u32 v[162:163], s[6:7], v171, v85, v[162:163]\n"
    "v_mad_u64_u32 v[164:165], s[6:7], v175, v85, v[164:165]\n"
    "ds_write2_b64 v207, v[168:169], v[170:171] offset0:0 offset1:146\n"
    "v_add_u32_dpp v166, v162, v162 row_half_mirror row_mask:0xf bank_mask:0x5\n"
    "v_add_u32_dpp v167, v163, v163 row_half_mirror row_mask:0xf bank_mask:0x5\n"
    "s_nop 0\n"
    "v_add_u32_dpp v166, v164, v164 row_half_mirror row_mask:0xf bank_mask:0xa\n"
    "v_add_u32_dpp v167, v165, v165 row_half_mirror row_mask:0xf bank_mask:0xa\n"
    "ds_write2_b64 v207, v[172:173], v[174:175] offset0:8 offset1:154\n"
    "v_add_u32_dpp v166, v166, v166 quad_perm:[1,0,3,2] row_mask:0xf bank_mask:0xf\n"
    "v_add_u32_dpp v167, v167, v167 quad_perm:[1,0,3,2] row_mask:0xf bank_mask:0xf\n"
    "ds_read_b128 v[180:183], v208\n"
    "ds_read_b128 v[184:187], v208 offset:576\n"
    "v_add_u32_dpp v166, v166, v166 quad_perm:[2,3,0,1] row_mask:0xf bank_mask:0xf\n"
    "v_add_u32_dpp v167, v167, v167 quad_perm:[2,3,0,1] row_mask:0xf bank_mask:0xf\n"
    "s_waitcnt vmcnt(0)\n"
    "v_cmp_ne_u64 vcc, v[166:167], v[176:177]\n"
    "s_cbranch_vccz Lr22x\n"
    "s_waitcnt vmcnt(0) lgkmcnt(0)\n"
    "s_sub_u32 s4, s4, 1\n"
    "s_cbranch_scc0 Lr22\n"
    "Lr22x:\n"
    "s_waitcnt vmcnt(0) lgkmcnt(0)\n"
    "s_nop 7\n"
    "s_branch Lsgy11_r\n"
    "Lspm13:\n"
    "s_waitcnt lgkmcnt(0)\n"
    "s_nop 7\n"
    "s_mov_b32 s4, 262144\n"
    "Lr23:\n"
    "buffer_load_dwordx4 v[30:33], off, s[20:23], s82 sc1\n"
    "s_waitcnt vmcnt(0)\n"
    "v_bitop3_b32 v8, v30, v32, s58 bitop3:0x96\n"
    "v_bitop3_b32 v9, v31, v33, s59 bitop3:0x96\n"
    "v_or_b32 v71, v8, v9\n"
    "v_cmp_ne_u32 vcc, 0, v71\n"
    "s_cbranch_vccz Lr23x\n"
    "s_waitcnt vmcnt(0) lgkmcnt(0)\n"
    "s_sub_u32 s4, s4, 1\n"
    "s_cbranch_scc0 Lr23\n"
    "Lr23x:\n"
    "s_waitcnt vmcnt(0) lgkmcnt(0)\n"
    "s_nop 7\n"
    "s_branch Lspm13_r\n"
    "Lspm15:\n"
    "s_waitcnt lgkmcnt(0)\n"
    "s_nop 7\n"
    "s_mov_b32 s4, 262144\n"
    "Lr24:\n"
    "buffer_load_dwordx4 v[34:37], off, s[20:23], s83 sc1\n"
    "s_waitcnt vmcnt(0)\n"
    "v_bitop3_b32 v8, v34, v36, s58 bitop3:0x96\n"
    "v_bitop3_b32 v9, v35, v37, s59 bitop3:0x96\n"
    "v_or_b32 v71, v8, v9\n"
    "v_cmp_ne_u32 vcc, 0, v71\n"
    "s_cbranch_vccz Lr24x\n"
    "s_waitcnt vmcnt(0) lgkmcnt(0)\n"
    "s_sub_u32 s4, s4, 1\n"
    "s_cbranch_scc0 Lr24\n"
    "Lr24x:\n"
    "s_waitcnt vmcnt(0) lgkmcnt(0)\n"
    "s_nop 7\n"
    "s_branch Lspm15_r\n"
    "Lsp0:\n"
    "s_waitcnt lgkmcnt(0)\n"
    "s_nop 7\n"
    "s_mov_b32 s4, 262144\n"
    "Lr25:\n"
    "buffer_load_dwordx4 v[38:41], v66, s[20:23], s73 offen offset:0 sc1\n"
    "s_waitcnt vmcnt(0)\n"
    "v_bitop3_b32 v8, v38, v40, s58 bitop3:0x96\n"
    "v_bitop3_b32 v9, v39, v41, s59 bitop3:0x96\n"
    "v_or_b32 v71, v8, v9\n"
    "v_cmp_ne_u32 vcc, 0, v71\n"
    "s_and_b32 vcc_hi, vcc_hi, 0x3fffff\n"
    "s_or_b32 s5, vcc_lo, vcc_hi\n"
    "s_cbranch_scc0 Lr25x\n"
    "s_waitcnt vmcnt(0) lgkmcnt(0)\n"
    "s_sub_u32 s4, s4, 1\n"
    "s_cbranch_scc0 Lr25\n"
    "Lr25x:\n"
    "s_waitcnt vmcnt(0) lgkmcnt(0)\n"
    "s_nop 7\n"
    "s_branch Lsp0_r\n"
    "Lsp1:\n"
    "s_waitcnt lgkmcnt(0)\n"
    "s_nop 7\n"
    "s_mov_b32 s4, 262144\n"
    "Lr26:\n"
    "buffer_load_dwordx4 v[42:45], v66, s[20:23], s73 offen offset:256 sc1\n"
    "s_waitcnt vmcnt(0)\n"
    "v_bitop3_b32 v8, v42, v44, s58 bitop3:0x96\n"
    "v_bitop3_b32 v9, v43, v45, s59 bitop3:0x96\n"
    "v_or_b32 v71, v8, v9\n"
    "v_cmp_ne_u32 vcc, 0, v71\n"
    "s_and_b32 vcc_hi, vcc_hi, 0x3fffff\n"
    "s_or_b32 s5, vcc_lo, vcc_hi\n"
    "s_cbranch_scc0 Lr26x\n"
    "s_waitcnt vmcnt(0) lgkmcnt(0)\n"
    "s_sub_u32 s4, s4, 1\n"
    "s_cbranch_scc0 Lr26\n"
    "Lr26x:\n"
    "s_waitcnt vmcnt(0) lgkmcnt(0)\n"
    "s_nop 7\n"
    "s_branch Lsp1_r\n"
    "Lstaty:\n"
    "s_add_u32 s65, s65, 32\n"
    "s_and_b32 s70, s3, 3\n"
    "s_lshl_b32 s4, s65, 2\n"
    "s_add_u32 s71, s4, s70\n"
    "s_mul_i32 s72, s71, 5120\n"
    "v_lshrrev_b32 v2, 4, v1\n"
    "v_and_b32 v3, 15, v1\n"
    "v_mul_u32_u24 v4, 1280, v2\n"
    "v_lshl_or_b32 v4, v3, 4, v4\n"
    "v_lshlrev_b32 v5, 4, v0\n"
    "Lstat:\n"
    "s_lshr_b32 s67, s3, 2\n"
    "s_sub_u32 s62, s61, 20\n"
    "s_lshr_b32 s4, s62, 1\n"
    "s_lshl_b32 s4, s4, 3\n"
    "s_add_u32 s4, s4, s60\n"
    "s_lshl_b32 s63, s4, 1\n"
    "s_and_b32 s4, s62, 1\n"
    "s_add_u32 s63, s63, s4\n"
    "s_cmp_lt_u32 s63, 54\n"
    "s_cselect_b64 s[68:69], -1, 0\n"
    "v_lshlrev_b32 v10, 4, v1\n"
    "v_lshlrev_b32 v7, 2, v1\n"
    "v_add_u32 v7, 152928, v7\n"
    "v_bfrev_b32 v11, 1\n"
    "v_cmp_gt_u32 vcc, 8, v1\n"
    "v_mov_b32 v9, 1\n"
    "v_mov_b32 v12, 154720\n"
    "v_cndmask_b32 v8, v11, v10, vcc\n"
    "s_cmp_lt_u32 s3, 4\n"
    "s_cbranch_scc1 Lold\n"
    "s_bitcmp1_b32 s3, 1\n"
    "s_cselect_b32 s4, 1065369472, 0\n"
    "s_cselect_b32 s5, 2320, 0\n"
    "s_cselect_b32 s6, 0, 37\n"
    "s_sub_u32 s5, 152928, s5\n"
    "s_add_u32 s6, s6, 79\n"
    "v_and_b32 v6, 127, v0\n"
    "v_min_u32 v6, s6, v6\n"
    "v_lshl_add_u32 v6, v6, 4, s5\n"
    "v_mov_b32 v14, s4\n"
    "v_mov_b32 v15, s4\n"
    "v_mov_b32 v16, s4\n"
    "v_mov_b32 v17, s4\n"
    "ds_write_b128 v6, v[14:17]\n"
    "s_waitcnt lgkmcnt(0)\n"
    "s_barrier\n"
    "s_load_dwordx2 s[56:57], s[20:21], 0x0\n"
    "s_cmp_lt_u32 s65, 96\n"
    "s_cbranch_scc1 Lnost\n"
    "buffer_load_dwordx4 v[18:21], v5, s[40:43], 0 offen\n"
    "buffer_load_dwordx4 v[22:25], v4, s[32:35], s72 offen offset:0\n"
    "buffer_load_dwordx4 v[26:29], v4, s[32:35], s72 offen offset:256\n"
    "buffer_load_dwordx4 v[30:33], v4, s[32:35], s72 offen offset:512\n"
    "buffer_load_dwordx4 v[34:37], v4, s[32:35], s72 offen offset:768\n"
    "buffer_load_dwordx4 v[38:41], v4, s[32:35], s72 offen offset:1024\n"
    "s_branch Lrows\n"
    "Lold:\n"
    "s_barrier\n"
    "Lrows:\n"
    "v_and_b32 v64, 7, v1\n"
    "v_and_b32 v6, 3, v1\n"
    "s_mov_b32 s6, 2027808452\n"
    "s_mov_b32 s7, 2246822519\n"
    "v_mul_lo_u32 v70, v6, s6\n"
    "v_add_u32 v76, 2654435761, v70\n"
    "v_add_u32 v77, 1013904226, v70\n"
    "v_add_u32 v78, 3668339987, v70\n"
    "v_add_u32 v79, 2027808452, v70\n"
    "v_lshrrev_b32 v70, 15, v76\n"
    "v_bitop3_b32 v76, v76, v70, 1 bitop3:0xbe\n"
    "v_lshrrev_b32 v70, 15, v77\n"
    "v_bitop3_b32 v77, v77, v70, 1 bitop3:0xbe\n"
    "v_lshrrev_b32 v70, 15, v78\n"
    "v_bitop3_b32 v78, v78, v70, 1 bitop3:0xbe\n"
    "v_lshrrev_b32 v70, 15, v79\n"
    "v_bitop3_b32 v79, v79, v70, 1 bitop3:0xbe\n"
    "v_cmp_lt_u32 vcc, 3, v64\n"
    "v_mov_b32 v70, s7\n"
    "s_nop 0\n"
    "v_cndmask_b32 v70, 1, v70, vcc\n"
    "v_mul_lo_u32 v76, v76, v70\n"
    "v_mul_lo_u32 v77, v77, v70\n"
    "v_mul_lo_u32 v78, v78, v70\n"
    "v_mul_lo_u32 v79, v79, v70\n"
    "s_mul_i32 s73, s67, 20480\n"
    "s_sub_u32 s73, 122880, s73\n"
    "s_lshl_b32 s4, s70, 1\n"
    "v_lshrrev_b32 v6, 1, v2\n"
    "v_add3_u32 v62, v6, v3, s4\n"
    "v_and_b32 v62, 7, v62\n"
    "v_lshlrev_b32 v62, 4, v62\n"
    "v_lshl_or_b32 v62, v3, 8, v62\n"
    "v_and_b32 v6, 1, v2\n"
    "v_lshl_or_b32 v62, v6, 3, v62\n"
    "v_add_u32 v62, s73, v62\n"
    "s_lshl_b32 s4, s3, 4\n"
    "s_add_u32 s4, s4, 154976\n"
    "v_lshl_add_u32 v63, v2, 2, s4\n"
    "v_cmp_eq_u32 s[76:77], 0, v64\n"
    "s_lshl_b32 s4, s67, 6\n"
    "s_add_u32 s4, s4, 154976\n"
    "v_lshl_add_u32 v67, v64, 3, s4\n"
    "v_lshrrev_b32 v74, 3, v1\n"
    "v_add_u32 v64, v64, v2\n"
    "v_and_b32 v64, 7, v64\n"
    "v_lshlrev_b32 v64, 4, v64\n"
    "v_lshl_or_b32 v64, v74, 7, v64\n"
    "s_lshl_b32 s4, s70, 11\n"
    "s_add_u32 s4, s4, s73\n"
    "v_add_u32 v64, s4, v64\n"
    "v_xor_b32 v65, 64, v64\n"
    "v_and_b32 v6, 4, v1\n"
    "v_lshl_add_u32 v66, v6, 1, v74\n"
    "v_lshlrev_b32 v66, 3, v66\n"
    "v_and_b32 v6, 3, v1\n"
    "v_cmp_eq_u32 vcc, 0, v6\n"
    "s_lshl_b32 s4, s71, 2\n"
    "s_sub_u32 s4, 2047, s4\n"
    "v_cndmask_b32 v66, v11, v66, vcc\n"
    "v_sub_u32 v68, s4, v2\n"
    "s_lshl_b32 s4, s67, 4\n"
    "s_add_u32 s4, s4, 154720\n"
    "v_mov_b32 v73, s4\n"
    "v_min_u32 v5, 1280, v5\n"
    "v_add_u32 v5, 155104, v5\n"
    "v_lshlrev_b32 v6, 4, v3\n"
    "v_add_u32 v6, 155104, v6\n"
    "s_lshl_b32 s74, s65, 7\n"
    "s_add_u32 s74, s74, 36864\n"
    "s_mul_i32 s75, s65, 10\n"
    "s_add_u32 s75, s75, s70\n"
    "s_mul_i32 s75, s75, 2176\n"
    "s_add_u32 s75, s75, 3139584\n"
    "s_add_u32 s79, s74, 24576\n"
    "s_lshl_b32 s74, s65, 4\n"
    "s_add_u32 s74, s74, 36864\n"
    "s_mov_b32 s78, 1065369472\n"
    "s_cmp_lt_u32 s3, 2\n"
    "s_cselect_b64 exec, -1, 0\n"
    "s_waitcnt vmcnt(5)\n"
    "ds_write_b128 v5, v[18:21]\n"
    "s_cmp_lt_u32 s3, 4\n"
    "s_cselect_b64 exec, -1, 0\n"
    "v_add_u32 v5, s3, v7\n"
    "ds_write_b8 v5, v9 offset:512\n"
    "s_mov_b64 exec, -1\n"
    "s_waitcnt lgkmcnt(0)\n"
    "s_mul_i32 s58, s56, 0x583678da\n"
    "s_mul_i32 s59, s57, 0xbe94f82a\n"
    "s_add_u32 s58, s58, 0x52f45a73\n"
    "s_add_u32 s59, s59, 0x370e6b27\n"
    "s_mov_b64 exec, 1\n"
    "ds_add_u32 v12, v9 offset:12\n"
    "s_mov_b64 exec, -1\n"
    "s_waitcnt lgkmcnt(0)\n"
    "s_mov_b32 s4, 4194304\n"
    "Lw27:\n"
    "ds_read_b32 v5, v7 offset:512\n"
    "s_waitcnt lgkmcnt(0)\n"
    "v_readfirstlane_b32 s5, v5\n"
    "s_cmp_eq_u32 s5, 0x01010101\n"
    "s_cbranch_scc1 Lw27x\n"
    "s_sub_u32 s4, s4, 1\n"
    "s_cbranch_scc0 Lw27\n"
    "Lw27x:\n"
    "ds_read_b128 v[42:45], v6 offset:0\n"
    "ds_read_b128 v[46:49], v6 offset:256\n"
    "ds_read_b128 v[50:53], v6 offset:512\n"
    "ds_read_b128 v[54:57], v6 offset:768\n"
    "ds_read_b128 v[58:61], v6 offset:1024\n"
    "v_mov_b32 v80, s58\n"
    "v_mov_b32 v81, s59\n"
    "v_cndmask_b32 v80, 0, v80, s[76:77]\n"
    "v_cndmask_b32 v81, 0, v81, s[76:77]\n"
    "s_waitcnt lgkmcnt(4)\n"
    "v_dot2_f32_bf16 v69, v42, s78, 0\n"
    "v_dot2_f32_bf16 v69, v43, s78, v69\n"
    "v_dot2_f32_bf16 v69, v44, s78, v69\n"
    "v_dot2_f32_bf16 v69, v45, s78, v69\n"
    "s_waitcnt lgkmcnt(3)\n"
    "v_dot2_f32_bf16 v69, v46, s78, v69\n"
    "v_dot2_f32_bf16 v69, v47, s78, v69\n"
    "v_dot2_f32_bf16 v69, v48, s78, v69\n"
    "v_dot2_f32_bf16 v69, v49, s78, v69\n"
    "s_waitcnt lgkmcnt(2)\n"
    "v_dot2_f32_bf16 v69, v50, s78, v69\n"
    "v_dot2_f32_bf16 v69, v51, s78, v69\n"
    "v_dot2_f32_bf16 v69, v52, s78, v69\n"
    "v_dot2_f32_bf16 v69, v53, s78, v69\n"
    "s_waitcnt lgkmcnt(1)\n"
    "v_dot2_f32_bf16 v69, v54, s78, v69\n"
    "v_dot2_f32_bf16 v69, v55, s78, v69\n"
    "v_dot2_f32_bf16 v69, v56, s78, v69\n"
    "v_dot2_f32_bf16 v69, v57, s78, v69\n"
    "s_waitcnt lgkmcnt(0)\n"
    "v_dot2_f32_bf16 v69, v58, s78, v69\n"
    "v_dot2_f32_bf16 v69, v59, s78, v69\n"
    "v_dot2_f32_bf16 v69, v60, s78, v69\n"
    "v_dot2_f32_bf16 v69, v61, s78, v69\n"
    "s_nop 2\n"
    "v_add_f32_dpp v69, v69, v69 quad_perm:[1,0,3,2] row_mask:0xf bank_mask:0xf\n"
    "s_nop 1\n"
    "v_add_f32_dpp v69, v69, v69 quad_perm:[2,3,0,1] row_mask:0xf bank_mask:0xf\n"
    "s_nop 1\n"
    "v_add_f32_dpp v69, v69, v69 row_half_mirror row_mask:0xf bank_mask:0xf\n"
    "s_nop 1\n"
    "v_add_f32_dpp v69, v69, v69 row_mirror row_mask:0xf bank_mask:0xf\n"
    "s_waitcnt vmcnt(4)\n"
    "v_dot2_f32_bf16 v70, v22, s78, 0\n"
    "v_dot2_f32_bf16 v71, v22, v22, 0\n"
    "v_dot2_f32_bf16 v72, v22, v42, 0\n"
    "v_dot2_f32_bf16 v70, v23, s78, v70\n"
    "v_dot2_f32_bf16 v71, v23, v23, v71\n"
    "v_dot2_f32_bf16 v72, v23, v43, v72\n"
    "v_dot2_f32_bf16 v70, v24, s78, v70\n"
    "v_dot2_f32_bf16 v71, v24, v24, v71\n"
    "v_dot2_f32_bf16 v72, v24, v44, v72\n"
    "v_dot2_f32_bf16 v70, v25, s78, v70\n"
    "v_dot2_f32_bf16 v71, v25, v25, v71\n"
    "v_dot2_f32_bf16 v72, v25, v45, v72\n"
    "s_waitcnt vmcnt(3)\n"
    "v_dot2_f32_bf16 v70, v26, s78, v70\n"
    "v_dot2_f32_bf16 v71, v26, v26, v71\n"
    "v_dot2_f32_bf16 v72, v26, v46, v72\n"
    "v_dot2_f32_bf16 v70, v27, s78, v70\n"
    "v_dot2_f32_bf16 v71, v27, v27, v71\n"
    "v_dot2_f32_bf16 v72, v27, v47, v72\n"
    "v_dot2_f32_bf16 v70, v28, s78, v70\n"
    "v_dot2_f32_bf16 v71, v28, v28, v71\n"
    "v_dot2_f32_bf16 v72, v28, v48, v72\n"
    "v_dot2_f32_bf16 v70, v29, s78, v70\n"
    "v_dot2_f32_bf16 v71, v29, v29, v71\n"
    "v_dot2_f32_bf16 v72, v29, v49, v72\n"
    "s_waitcnt vmcnt(2)\n"
    "v_dot2_f32_bf16 v70, v30, s78, v70\n"
    "v_dot2_f32_bf16 v71, v30, v30, v71\n"
    "v_dot2_f32_bf16 v72, v30, v50, v72\n"
    "v_dot2_f32_bf16 v70, v31, s78, v70\n"
    "v_dot2_f32_bf16 v71, v31, v31, v71\n"
    "v_dot2_f32_bf16 v72, v31, v51, v72\n"
    "v_dot2_f32_bf16 v70, v32, s78, v70\n"
    "v_dot2_f32_bf16 v71, v32, v32, v71\n"
    "v_dot2_f32_bf16 v72, v32, v52, v72\n"
    "v_dot2_f32_bf16 v70, v33, s78, v70\n"
    "v_dot2_f32_bf16 v71, v33, v33, v71\n"
    "v_dot2_f32_bf16 v72, v33, v53, v72\n"
    "s_waitcnt vmcnt(1)\n"
    "v_dot2_f32_bf16 v70, v34, s78, v70\n"
    "v_dot2_f32_bf16 v71, v34, v34, v71\n"
    "v_dot2_f32_bf16 v72, v34, v54, v72\n"
    "v_dot2_f32_bf16 v70, v35, s78, v70\n"
    "v_dot2_f32_bf16 v71, v35, v35, v71\n"
    "v_dot2_f32_bf16 v72, v35, v55, v72\n"
    "v_dot2_f32_bf16 v70, v36, s78, v70\n"
    "v_dot2_f32_bf16 v71, v36, v36, v71\n"
    "v_dot2_f32_bf16 v72, v36, v56, v72\n"
    "v_dot2_f32_bf16 v70, v37, s78, v70\n"
    "v_dot2_f32_bf16 v71, v37, v37, v71\n"
    "v_dot2_f32_bf16 v72, v37, v57, v72\n"
    "s_waitcnt vmcnt(0)\n"
    "v_dot2_f32_bf16 v70, v38, s78, v70\n"
    "v_dot2_f32_bf16 v71, v38, v38, v71\n"
    "v_dot2_f32_bf16 v72, v38, v58, v72\n"
    "v_dot2_f32_bf16 v70, v39, s78, v70\n"
    "v_dot2_f32_bf16 v71, v39, v39, v71\n"
    "v_dot2_f32_bf16 v72, v39, v59, v72\n"
    "v_dot2_f32_bf16 v70, v40, s78, v70\n"
    "v_dot2_f32_bf16 v71, v40, v40, v71\n"
    "v_dot2_f32_bf16 v72, v40, v60, v72\n"
    "v_dot2_f32_bf16 v70, v41, s78, v70\n"
    "v_dot2_f32_bf16 v71, v41, v41, v71\n"
    "v_dot2_f32_bf16 v72, v41, v61, v72\n"
    "s_nop 0\n"
    "v_add_f32_dpp v70, v70, v70 quad_perm:[1,0,3,2] row_mask:0xf bank_mask:0xf\n"
    "v_add_f32_dpp v71, v71, v71 quad_perm:[1,0,3,2] row_mask:0xf bank_mask:0xf\n"
    "v_add_f32_dpp v72, v72, v72 quad_perm:[1,0,3,2] row_mask:0xf bank_mask:0xf\n"
    "v_add_f32_dpp v70, v70, v70 quad_perm:[2,3,0,1] row_mask:0xf bank_mask:0xf\n"
    "v_add_f32_dpp v71, v71, v71 quad_perm:[2,3,0,1] row_mask:0xf bank_mask:0xf\n"
    "v_add_f32_dpp v72, v72, v72 quad_perm:[2,3,0,1] row_mask:0xf bank_mask:0xf\n"
    "v_add_f32_dpp v70, v70, v70 row_half_mirror row_mask:0xf bank_mask:0xf\n"
    "v_add_f32_dpp v71, v71, v71 row_half_mirror row_mask:0xf bank_mask:0xf\n"
    "v_add_f32_dpp v72, v72, v72 row_half_mirror row_mask:0xf bank_mask:0xf\n"
    "v_add_f32_dpp v70, v70, v70 row_mirror row_mask:0xf bank_mask:0xf\n"
    "v_add_f32_dpp v71, v71, v71 row_mirror row_mask:0xf bank_mask:0xf\n"
    "v_add_f32_dpp v72, v72, v72 row_mirror row_mask:0xf bank_mask:0xf\n"
    "v_mul_f32 v70, 0x3acccccd, v70\n"
    "v_mul_f32 v71, 0x3acccccd, v71\n"
    "v_fma_f32 v71, -v70, v70, v71\n"
    "v_max_f32 v71, 0, v71\n"
    "v_cvt_pk_bf16_f32 v71, 0, v71\n"
    "v_add_f32 v71, 0x3727c5ac, v71\n"
    "v_cvt_pk_bf16_f32 v71, 0, v71\n"
    "v_sqrt_f32 v71, v71\n"
    "v_cvt_pk_bf16_f32 v5, 0, v70\n"
    "v_cvt_pk_bf16_f32 v70, v70, v71\n"
    "v_fma_f32 v72, -v5, v69, v72\n"
    "v_and_b32 v71, 0xffff0000, v70\n"
    "v_rcp_f32 v71, v71\n"
    "s_mov_b32 s4, 0x00010001\n"
    "s_mov_b32 s5, s4\n"
    "v_mul_f32 v72, v72, v71\n"
    "v_ashrrev_i32 v5, 31, v72\n"
    "v_cmp_o_f32 vcc, v72, v72\n"
    "v_or_b32 v5, v11, v5\n"
    "v_xor_b32 v5, v5, v72\n"
    "v_cndmask_b32 v5, 0, v5, vcc\n"
    "v_and_b32 v5, 0xfffff800, v5\n"
    "v_or_b32 v5, v5, v68\n"
    "s_mov_b64 exec, s[4:5]\n"
    "ds_max_rtn_u32 v18, v73, v5\n"
    "ds_write_b32 v63, v70\n"
    "s_waitcnt lgkmcnt(1)\n"
    "v_min_u32 v18, v18, v5\n"
    "ds_max_u32 v73, v18 offset:4\n"
    "s_mov_b64 exec, 1\n"
    "ds_add_rtn_u32 v18, v73, v9 offset:8\n"
    "s_mov_b64 exec, -1\n"
    "ds_read_b64 v[14:15], v73\n"
    "ds_read_b64 v[42:43], v67\n"
    "s_waitcnt lgkmcnt(2)\n"
    "v_readfirstlane_b32 s5, v18\n"
    "s_cmp_lg_u32 s5, 3\n"
    "s_cbranch_scc1 Lnotlast\n"
    "s_waitcnt lgkmcnt(1)\n"
    "v_xor_b32 v16, s58, v14\n"
    "v_xor_b32 v17, s59, v15\n"
    "s_mov_b64 exec, 1\n"
    "buffer_store_dwordx4 v[14:17], v8, s[20:23], s74 offen sc1\n"
    "s_mov_b64 exec, -1\n"
    "s_waitcnt lgkmcnt(0)\n"
    "v_xor_b32 v44, s58, v42\n"
    "v_xor_b32 v45, s59, v43\n"
    "buffer_store_dwordx4 v[42:45], v8, s[20:23], s79 offen sc1\n"
    "Lnotlast:\n"
    "s_waitcnt lgkmcnt(0)\n"
    "s_cmp_lg_u32 s3, 2\n"
    "s_cbranch_scc1 La28y\n"
    "s_mov_b32 s4, 4194304\n"
    "La28:\n"
    "ds_read_b32 v5, v12 offset:12\n"
    "s_waitcnt lgkmcnt(0)\n"
    "v_readfirstlane_b32 s5, v5\n"
    "s_cmp_ge_u32 s5, 8\n"
    "s_cbranch_scc1 La28x\n"
    "s_sub_u32 s4, s4, 1\n"
    "s_cbranch_scc0 La28\n"
    "La28x:\n"
    "v_mov_b32 v14, s58\n"
    "v_mov_b32 v15, s59\n"
    "v_mov_b32 v16, s58\n"
    "v_mov_b32 v17, s59\n"
    "s_lshl_b32 s4, s2, 7\n"
    "s_add_u32 s4, s4, 4096\n"
    "buffer_store_dwordx4 v[14:17], v8, s[20:23], s4 offen sc1\n"
    "s_nop 1\n"
    "La28y:\n"
    "s_and_b64 vcc, s[68:69], exec\n"
    "s_cbranch_vccnz Lffn\n"
    "ds_write_b64 v62, v[22:23] offset:0\n"
    "ds_write_b64 v62, v[24:25] offset:128\n"
    "ds_write_b64 v62, v[26:27] offset:4096\n"
    "ds_write_b64 v62, v[28:29] offset:4224\n"
    "ds_write_b64 v62, v[30:31] offset:8192\n"
    "ds_write_b64 v62, v[32:33] offset:8320\n"
    "ds_write_b64 v62, v[34:35] offset:12288\n"
    "ds_write_b64 v62, v[36:37] offset:12416\n"
    "ds_write_b64 v62, v[38:39] offset:16384\n"
    "ds_write_b64 v62, v[40:41] offset:16512\n"
    "s_mov_b32 s80, s75\n"
    "s_waitcnt lgkmcnt(0)\n"
    "Lspare:\n"
    "v_lshlrev_b32 v6, 7, v1\n"
    "s_barrier\n"
    "s_cmp_lt_u32 s60, 3\n"
    "s_cselect_b32 s71, 4, 3\n"
    "s_cselect_b32 s72, 4, 6\n"
    "s_mul_i32 s73, s3, s72\n"
    "s_add_u32 s73, s73, 11\n"
    "s_sub_u32 s73, s73, s62\n"
    "s_lshl_b32 s72, s72, 3\n"
    "s_mul_i32 s74, s71, 10\n"
    "s_lshl_b32 s4, s67, 5\n"
    "s_sub_u32 s4, s65, s4\n"
    "s_cmp_lt_u32 s4, 64\n"
    "s_cbranch_scc1 Lxdone\n"
    "ds_read_b128 v[22:25], v64 offset:0\n"
    "ds_read_b128 v[46:49], v65 offset:1024\n"
    "ds_read_b128 v[26:29], v64 offset:8192\n"
    "ds_read_b128 v[50:53], v65 offset:9216\n"
    "ds_read_b128 v[30:33], v64 offset:16384\n"
    "ds_read_b128 v[54:57], v65 offset:17408\n"
    "s_waitcnt lgkmcnt(5)\n"
    "buffer_store_dwordx4 v[22:25], v10, s[20:23], s80 offen sc1\n"
    "s_waitcnt lgkmcnt(4)\n"
    "buffer_store_dwordx4 v[46:49], v10, s[20:23], s80 offen offset:1024 sc1\n"
    "v_mad_u64_u32 v[82:83], s[6:7], v22, v76, v[80:81]\n"
    "v_mad_u64_u32 v[84:85], s[6:7], v46, v76, v[80:81]\n"
    "v_mad_u64_u32 v[82:83], s[6:7], v23, v77, v[82:83]\n"
    "v_mad_u64_u32 v[84:85], s[6:7], v47, v77, v[84:85]\n"
    "v_mad_u64_u32 v[82:83], s[6:7], v24, v78, v[82:83]\n"
    "v_mad_u64_u32 v[84:85], s[6:7], v48, v78, v[84:85]\n"
    "v_mad_u64_u32 v[82:83], s[6:7], v25, v79, v[82:83]\n"
    "v_mad_u64_u32 v[84:85], s[6:7], v49, v79, v[84:85]\n"
    "s_nop 0\n"
    "v_add_u32_dpp v86, v82, v82 row_half_mirror row_mask:0xf bank_mask:0x5\n"
    "v_add_u32_dpp v87, v83, v83 row_half_mirror row_mask:0xf bank_mask:0x5\n"
    "s_nop 0\n"
    "v_add_u32_dpp v86, v84, v84 row_half_mirror row_mask:0xf bank_mask:0xa\n"
    "v_add_u32_dpp v87, v85, v85 row_half_mirror row_mask:0xf bank_mask:0xa\n"
    "s_nop 0\n"
    "v_add_u32_dpp v86, v86, v86 quad_perm:[1,0,3,2] row_mask:0xf bank_mask:0xf\n"
    "v_add_u32_dpp v87, v87, v87 quad_perm:[1,0,3,2] row_mask:0xf bank_mask:0xf\n"
    "s_nop 0\n"
    "v_add_u32_dpp v86, v86, v86 quad_perm:[2,3,0,1] row_mask:0xf bank_mask:0xf\n"
    "v_add_u32_dpp v87, v87, v87 quad_perm:[2,3,0,1] row_mask:0xf bank_mask:0xf\n"
    "buffer_store_dwordx2 v[86:87], v66, s[20:23], s80 offen offset:2048 sc1\n"
    "s_add_u32 s80, s80, 8704\n"
    "s_waitcnt lgkmcnt(3)\n"
    "buffer_store_dwordx4 v[26:29], v10, s[20:23], s80 offen sc1\n"
    "s_waitcnt lgkmcnt(2)\n"
    "buffer_store_dwordx4 v[50:53], v10, s[20:23], s80 offen offset:1024 sc1\n"
    "v_mad_u64_u32 v[82:83], s[6:7], v26, v76, v[80:81]\n"
    "v_mad_u64_u32 v[84:85], s[6:7], v50, v76, v[80:81]\n"
    "v_mad_u64_u32 v[82:83], s[6:7], v27, v77, v[82:83]\n"
    "v_mad_u64_u32 v[84:85], s[6:7], v51, v77, v[84:85]\n"
    "v_mad_u64_u32 v[82:83], s[6:7], v28, v78, v[82:83]\n"
    "v_mad_u64_u32 v[84:85], s[6:7], v52, v78, v[84:85]\n"
    "v_mad_u64_u32 v[82:83], s[6:7], v29, v79, v[82:83]\n"
    "v_mad_u64_u32 v[84:85], s[6:7], v53, v79, v[84:85]\n"
    "s_nop 0\n"
    "v_add_u32_dpp v86, v82, v82 row_half_mirror row_mask:0xf bank_mask:0x5\n"
    "v_add_u32_dpp v87, v83, v83 row_half_mirror row_mask:0xf bank_mask:0x5\n"
    "s_nop 0\n"
    "v_add_u32_dpp v86, v84, v84 row_half_mirror row_mask:0xf bank_mask:0xa\n"
    "v_add_u32_dpp v87, v85, v85 row_half_mirror row_mask:0xf bank_mask:0xa\n"
    "s_nop 0\n"
    "v_add_u32_dpp v86, v86, v86 quad_perm:[1,0,3,2] row_mask:0xf bank_mask:0xf\n"
    "v_add_u32_dpp v87, v87, v87 quad_perm:[1,0,3,2] row_mask:0xf bank_mask:0xf\n"
    "s_nop 0\n"
    "v_add_u32_dpp v86, v86, v86 quad_perm:[2,3,0,1] row_mask:0xf bank_mask:0xf\n"
    "v_add_u32_dpp v87, v87, v87 quad_perm:[2,3,0,1] row_mask:0xf bank_mask:0xf\n"
    "buffer_store_dwordx2 v[86:87], v66, s[20:23], s80 offen offset:2048 sc1\n"
    "s_add_u32 s80, s80, 8704\n"
    "s_cmp_ge_u32 s70, 2\n"
    "s_cbranch_scc1 Lxdone\n"
    "s_waitcnt lgkmcnt(1)\n"
    "buffer_store_dwordx4 v[30:33], v10, s[20:23], s80 offen sc1\n"
    "s_waitcnt lgkmcnt(0)\n"
    "buffer_store_dwordx4 v[54:57], v10, s[20:23], s80 offen offset:1024 sc1\n"
    "v_mad_u64_u32 v[82:83], s[6:7], v30, v76, v[80:81]\n"
    "v_mad_u64_u32 v[84:85], s[6:7], v54, v76, v[80:81]\n"
    "v_mad_u64_u32 v[82:83], s[6:7], v31, v77, v[82:83]\n"
    "v_mad_u64_u32 v[84:85], s[6:7], v55, v77, v[84:85]\n"
    "v_mad_u64_u32 v[82:83], s[6:7], v32, v78, v[82:83]\n"
    "v_mad_u64_u32 v[84:85], s[6:7], v56, v78, v[84:85]\n"
    "v_mad_u64_u32 v[82:83], s[6:7], v33, v79, v[82:83]\n"
    "v_mad_u64_u32 v[84:85], s[6:7], v57, v79, v[84:85]\n"
    "s_nop 0\n"
    "v_add_u32_dpp v86, v82, v82 row_half_mirror row_mask:0xf bank_mask:0x5\n"
    "v_add_u32_dpp v87, v83, v83 row_half_mirror row_mask:0xf bank_mask:0x5\n"
    "s_nop 0\n"
    "v_add_u32_dpp v86, v84, v84 row_half_mirror row_mask:0xf bank_mask:0xa\n"
    "v_add_u32_dpp v87, v85, v85 row_half_mirror row_mask:0xf bank_mask:0xa\n"
    "s_nop 0\n"
    "v_add_u32_dpp v86, v86, v86 quad_perm:[1,0,3,2] row_mask:0xf bank_mask:0xf\n"
    "v_add_u32_dpp v87, v87, v87 quad_perm:[1,0,3,2] row_mask:0xf bank_mask:0xf\n"
    "s_nop 0\n"
    "v_add_u32_dpp v86, v86, v86 quad_perm:[2,3,0,1] row_mask:0xf bank_mask:0xf\n"
    "v_add_u32_dpp v87, v87, v87 quad_perm:[2,3,0,1] row_mask:0xf bank_mask:0xf\n"
    "buffer_store_dwordx2 v[86:87], v66, s[20:23], s80 offen offset:2048 sc1\n"
    "s_add_u32 s80, s80, 8704\n"
    "Lxdone:\n"
    "s_waitcnt lgkmcnt(0)\n"
    "s_barrier\n"
    "Lpf3:\n"
    "s_cmp_ge_u32 s73, s74\n"
    "s_cbranch_scc1 Lpfx\n"
    "s_mul_i32 s5, s73, 6554\n"
    "s_lshr_b32 s5, s5, 16\n"
    "s_mul_i32 s4, s5, 10\n"
    "s_sub_u32 s4, s73, s4\n"
    "s_lshl_b32 s4, s4, 13\n"
    "s_lshl_b32 s5, s5, 3\n"
    "s_add_u32 s5, s5, s60\n"
    "s_mul_i32 s5, s5, 81920\n"
    "s_add_u32 s5, s5, s4\n"
    "buffer_load_dword v6, s[52:55], s5 offen lds\n"
    "s_add_u32 s73, s73, s72\n"
    "s_branch Lpf3\n"
    "Lpfx:\n"
    "s_lshl_b32 s4, s2, 3\n"
    "s_add_u32 s4, s4, s3\n"
    "s_cmp_lg_u32 s4, 2047\n"
    "s_cbranch_scc1 Lfin\n"
    "s_waitcnt vmcnt(0)\n"
    "s_mov_b32 s4, 262144\n"
    "Lr29:\n"
    "s_sleep 8\n"
    "s_mov_b32 s5, 4096\n"
    "buffer_load_dwordx2 v[22:23], v6, s[20:23], s5 offen sc1\n"
    "s_mov_b32 s5, 12288\n"
    "buffer_load_dwordx2 v[26:27], v6, s[20:23], s5 offen sc1\n"
    "s_mov_b32 s5, 20480\n"
    "buffer_load_dwordx2 v[30:31], v6, s[20:23], s5 offen sc1\n"
    "s_mov_b32 s5, 28672\n"
    "buffer_load_dwordx2 v[34:35], v6, s[20:23], s5 offen sc1\n"
    "s_waitcnt vmcnt(3)\n"
    "v_cmp_ne_u64 vcc, s[58:59], v[22:23]\n"
    "s_waitcnt vmcnt(2)\n"
    "v_cmp_ne_u64 s[6:7], s[58:59], v[26:27]\n"
    "s_or_b64 vcc, vcc, s[6:7]\n"
    "s_waitcnt vmcnt(1)\n"
    "v_cmp_ne_u64 s[6:7], s[58:59], v[30:31]\n"
    "s_or_b64 vcc, vcc, s[6:7]\n"
    "s_waitcnt vmcnt(0)\n"
    "v_cmp_ne_u64 s[6:7], s[58:59], v[34:35]\n"
    "s_or_b64 vcc, vcc, s[6:7]\n"
    "s_cbranch_vccz Lr29x\n"
    "s_waitcnt vmcnt(0) lgkmcnt(0)\n"
    "s_sub_u32 s4, s4, 1\n"
    "s_cbranch_scc0 Lr29\n"
    "Lr29x:\n"
    "s_add_u32 s56, s56, 1\n"
    "s_add_u32 s57, s57, 1\n"
    "v_mov_b32 v14, s56\n"
    "v_mov_b32 v15, s57\n"
    "s_mov_b64 exec, 1\n"
    "buffer_store_dwordx2 v[14:15], off, s[20:23], 0 sc1\n"
    "s_branch Lfin\n"
    "Lnost:\n"
    "s_and_b64 vcc, s[68:69], exec\n"
    "s_cbranch_vccz Lidle\n"
    "s_and_b32 s71, s3, 1\n"
    "s_bfe_u32 s72, s3, 0x10001\n"
    "v_lshrrev_b32 v192, 2, v1\n"
    "v_mul_u32_u24 v192, 3456, v192\n"
    "v_and_b32 v5, 2, v2\n"
    "v_xor_b32 v5, v5, v1\n"
    "v_and_b32 v5, 3, v5\n"
    "v_lshl_add_u32 v192, v5, 4, v192\n"
    "s_and_b32 s4, s63, 1\n"
    "s_mul_i32 s73, s4, 20\n"
    "s_sub_u32 s74, 20, s73\n"
    "s_add_u32 s73, s73, s72\n"
    "s_add_u32 s74, s74, s72\n"
    "s_lshl_b32 s5, s63, 6\n"
    "s_mul_i32 s75, s73, 55296\n"
    "s_add_u32 s75, s75, s5\n"
    "s_mul_i32 s76, s74, 55296\n"
    "s_add_u32 s76, s76, s5\n"
    "s_cmp_eq_u32 s71, 1\n"
    "s_cselect_b64 s[44:45], s[48:49], s[44:45]\n"
    "buffer_load_dwordx4 v[196:199], v192, s[44:47], s75 offen\n"
    "s_add_u32 s75, s75, 110592\n"
    "buffer_load_dwordx4 v[200:203], v192, s[44:47], s75 offen\n"
    "s_add_u32 s75, s75, 110592\n"
    "s_mul_i32 s4, s71, 40960\n"
    "s_lshl_b32 s5, s73, 10\n"
    "s_add_u32 s5, s5, s4\n"
    "v_add_u32 v193, s5, v10\n"
    "s_lshl_b32 s5, s74, 10\n"
    "s_add_u32 s5, s5, s4\n"
    "v_add_u32 v194, s5, v10\n"
    "v_add_u32 v195, s70, v7\n"
    "s_and_b32 s82, s3, 1\n"
    "s_lshr_b32 s83, s3, 1\n"
    "s_and_b32 s4, s63, 1\n"
    "s_lshl_b32 s4, s4, 1\n"
    "s_xor_b32 s83, s83, s4\n"
    "v_bfe_u32 v27, v1, 2, 2\n"
    "v_and_b32 v28, 3, v1\n"
    "v_lshl_add_u32 v5, v2, 3, v27\n"
    "v_and_b32 v6, 1, v2\n"
    "v_xor_b32 v18, s82, v6\n"
    "v_lshlrev_b32 v18, 5, v18\n"
    "v_lshl_or_b32 v18, v28, 3, v18\n"
    "v_lshl_add_u32 v18, v5, 6, v18\n"
    "s_mul_i32 s4, s83, 10240\n"
    "v_add_u32 v18, s4, v18\n"
    "v_min_u32 v19, 2, v3\n"
    "v_mul_u32_u24 v19, 1296, v19\n"
    "v_lshl_add_u32 v19, v2, 4, v19\n"
    "s_mul_i32 s4, s83, 320\n"
    "s_add_u32 s4, s4, 148016\n"
    "v_add_u32 v19, s4, v19\n"
    "v_mul_u32_u24 v20, 1552, v3\n"
    "v_mul_u32_u24 v21, 192, v2\n"
    "v_add_u32 v20, v20, v21\n"
    "s_mul_i32 s4, s82, 768\n"
    "s_lshl_b32 s5, s83, 2\n"
    "s_add_u32 s4, s4, s5\n"
    "s_add_u32 s4, s4, 143360\n"
    "v_add_u32 v20, s4, v20\n"
    "v_and_b32 v22, 31, v1\n"
    "v_mul_u32_u24 v22, 48, v22\n"
    "v_lshrrev_b32 v23, 5, v1\n"
    "v_mul_u32_u24 v21, 1552, v23\n"
    "v_add_u32 v21, v21, v22\n"
    "v_add_u32 v21, 143360, v21\n"
    "v_add_u32 v22, 146464, v22\n"
    "v_lshlrev_b32 v23, 4, v23\n"
    "v_add_u32 v23, 154768, v23\n"
    "s_lshl_b32 s4, s3, 7\n"
    "s_add_u32 s4, s4, 151904\n"
    "v_lshl_add_u32 v24, v1, 1, s4\n"
    "v_bfe_u32 v25, v1, 2, 1\n"
    "v_lshl_add_u32 v25, v25, 6, s4\n"
    "v_lshl_add_u32 v25, v2, 4, v25\n"
    "s_sub_u32 s4, s3, 3\n"
    "s_cselect_b32 s4, s3, s4\n"
    "s_mul_i32 s84, s63, 5120\n"
    "s_lshl_b32 s5, s4, 10\n"
    "s_add_u32 s84, s84, s5\n"
    "s_add_u32 s84, s84, 77824\n"
    "s_mul_i32 s85, s63, 40960\n"
    "s_lshl_b32 s5, s4, 8\n"
    "s_add_u32 s85, s85, s5\n"
    "s_add_u32 s86, s85, 5120\n"
    "v_mul_u32_u24 v31, 10240, v2\n"
    "v_lshl_add_u32 v31, v3, 4, v31\n"
    "s_mov_b32 s87, 0x05040100\n"
    "s_mov_b32 s88, 0x07060302\n"
    "s_waitcnt lgkmcnt(0)\n"
    "s_mul_i32 s58, s56, 0x583678da\n"
    "s_mul_i32 s59, s57, 0xbe94f82a\n"
    "s_add_u32 s58, s58, 0x52f45a73\n"
    "s_add_u32 s59, s59, 0x370e6b27\n"
    "s_mov_b64 exec, 1\n"
    "ds_add_u32 v12, v9 offset:12\n"
    "s_mov_b64 exec, -1\n"
    "buffer_load_dwordx4 v[204:207], v192, s[44:47], s75 offen\n"
    "s_add_u32 s75, s75, 110592\n"
    "s_waitcnt vmcnt(2)\n"
    "ds_write_b128 v193, v[196:199] offset:0\n"
    "buffer_load_dwordx4 v[208:211], v192, s[44:47], s75 offen\n"
    "s_add_u32 s75, s75, 110592\n"
    "s_waitcnt vmcnt(2)\n"
    "ds_write_b128 v193, v[200:203] offset:2048\n"
    "buffer_load_dwordx4 v[212:215], v192, s[44:47], s75 offen\n"
    "s_add_u32 s75, s75, 110592\n"
    "s_waitcnt vmcnt(2)\n"
    "ds_write_b128 v193, v[204:207] offset:4096\n"
    "buffer_load_dwordx4 v[196:199], v192, s[44:47], s75 offen\n"
    "s_add_u32 s75, s75, 110592\n"
    "s_waitcnt vmcnt(2)\n"
    "ds_write_b128 v193, v[208:211] offset:6144\n"
    "buffer_load_dwordx4 v[200:203], v192, s[44:47], s75 offen\n"
    "s_add_u32 s75, s75, 110592\n"
    "s_waitcnt vmcnt(2)\n"
    "ds_write_b128 v193, v[212:215] offset:8192\n"
    "buffer_load_dwordx4 v[204:207], v192, s[44:47], s75 offen\n"
    "s_add_u32 s75, s75, 110592\n"
    "ds_write_b8 v195, v9 offset:256\n"
    "s_waitcnt vmcnt(2)\n"
    "ds_write_b128 v193, v[196:199] offset:10240\n"
    "buffer_load_dwordx4 v[208:211], v192, s[44:47], s75 offen\n"
    "s_add_u32 s75, s75, 110592\n"
    "s_waitcnt vmcnt(2)\n"
    "ds_write_b128 v193, v[200:203] offset:12288\n"
    "buffer_load_dwordx4 v[212:215], v192, s[44:47], s75 offen\n"
    "s_add_u32 s75, s75, 110592\n"
    "buffer_load_dwordx4 v[196:199], v192, s[44:47], s76 offen\n"
    "s_add_u32 s76, s76, 110592\n"
    "s_waitcnt vmcnt(3)\n"
    "ds_write_b128 v193, v[204:207] offset:14336\n"
    "buffer_load_dwordx4 v[200:203], v192, s[44:47], s76 offen\n"
    "s_add_u32 s76, s76, 110592\n"
    "s_waitcnt vmcnt(3)\n"
    "ds_write_b128 v193, v[208:211] offset:16384\n"
    "buffer_load_dwordx4 v[204:207], v192, s[44:47], s76 offen\n"
    "s_add_u32 s76, s76, 110592\n"
    "s_waitcnt vmcnt(3)\n"
    "ds_write_b128 v193, v[212:215] offset:18432\n"
    "ds_write_b8 v195, v9 offset:768\n"
    "buffer_load_dwordx4 v[208:211], v192, s[44:47], s76 offen\n"
    "s_add_u32 s76, s76, 110592\n"
    "s_waitcnt vmcnt(3)\n"
    "ds_write_b128 v194, v[196:199] offset:0\n"
    "buffer_load_dwordx4 v[212:215], v192, s[44:47], s76 offen\n"
    "s_add_u32 s76, s76, 110592\n"
    "s_waitcnt vmcnt(3)\n"
    "ds_write_b128 v194, v[200:203] offset:2048\n"
    "buffer_load_dwordx4 v[196:199], v192, s[44:47], s76 offen\n"
    "s_add_u32 s76, s76, 110592\n"
    "s_waitcnt vmcnt(3)\n"
    "ds_write_b128 v194, v[204:207] offset:4096\n"
    "buffer_load_dwordx4 v[200:203], v192, s[44:47], s76 offen\n"
    "s_add_u32 s76, s76, 110592\n"
    "s_waitcnt vmcnt(3)\n"
    "ds_write_b128 v194, v[208:211] offset:6144\n"
    "buffer_load_dwordx4 v[204:207], v192, s[44:47], s76 offen\n"
    "s_add_u32 s76, s76, 110592\n"
    "s_waitcnt vmcnt(3)\n"
    "ds_write_b128 v194, v[212:215] offset:8192\n"
    "buffer_load_dwordx4 v[208:211], v192, s[44:47], s76 offen\n"
    "s_add_u32 s76, s76, 110592\n"
    "s_waitcnt vmcnt(3)\n"
    "ds_write_b128 v194, v[196:199] offset:10240\n"
    "buffer_load_dwordx4 v[212:215], v192, s[44:47], s76 offen\n"
    "s_add_u32 s76, s76, 110592\n"
    "ds_write_b8 v195, v9 offset:1024\n"
    "s_cmp_lt_u32 s3, 6\n"
    "s_cbranch_scc1 Lnd7\n"
    "s_waitcnt vmcnt(3)\n"
    "ds_write_b128 v194, v[200:203] offset:12288\n"
    "s_waitcnt vmcnt(2)\n"
    "ds_write_b128 v194, v[204:207] offset:14336\n"
    "s_waitcnt lgkmcnt(0)\n"
    "s_mov_b32 s4, 4194304\n"
    "Lw30:\n"
    "ds_read_b32 v5, v7 offset:1024\n"
    "s_waitcnt lgkmcnt(0)\n"
    "v_readfirstlane_b32 s5, v5\n"
    "s_cmp_eq_u32 s5, 0x01010101\n"
    "s_cbranch_scc1 Lw30x\n"
    "s_sub_u32 s4, s4, 1\n"
    "s_cbranch_scc0 Lw30\n"
    "Lw30x:\n"
    "buffer_load_dwordx4 v[128:131], v31, s[52:55], s85 offen offset:0\n"
    "buffer_load_dwordx4 v[132:135], v31, s[52:55], s85 offen offset:1280\n"
    "buffer_load_dwordx4 v[136:139], v31, s[52:55], s85 offen offset:2560\n"
    "buffer_load_dwordx4 v[140:143], v31, s[52:55], s85 offen offset:3840\n"
    "buffer_load_dwordx4 v[144:147], v31, s[52:55], s86 offen offset:0\n"
    "buffer_load_dwordx4 v[148:151], v31, s[52:55], s86 offen offset:1280\n"
    "buffer_load_dwordx4 v[152:155], v31, s[52:55], s86 offen offset:2560\n"
    "buffer_load_dwordx4 v[156:159], v31, s[52:55], s86 offen offset:3840\n"
    "s_waitcnt vmcnt(9)\n"
    "ds_write_b128 v194, v[208:211] offset:16384\n"
    "s_waitcnt vmcnt(8)\n"
    "ds_write_b128 v194, v[212:215] offset:18432\n"
    "s_branch Lgu\n"
    "Lnd7:\n"
    "s_waitcnt vmcnt(3)\n"
    "ds_write_b128 v194, v[200:203] offset:12288\n"
    "s_waitcnt vmcnt(2)\n"
    "ds_write_b128 v194, v[204:207] offset:14336\n"
    "s_waitcnt vmcnt(1)\n"
    "ds_write_b128 v194, v[208:211] offset:16384\n"
    "s_waitcnt vmcnt(0)\n"
    "ds_write_b128 v194, v[212:215] offset:18432\n"
    "Lgu:\n"
    "ds_write_b8 v195, v9\n"
    "s_waitcnt lgkmcnt(0)\n"
    "s_mov_b32 s4, 4194304\n"
    "Lw31:\n"
    "ds_read_b32 v5, v7 offset:0\n"
    "s_waitcnt lgkmcnt(0)\n"
    "v_readfirstlane_b32 s5, v5\n"
    "s_cmp_eq_u32 s5, 0x01010101\n"
    "s_cbranch_scc1 Lw31x\n"
    "s_sub_u32 s4, s4, 1\n"
    "s_cbranch_scc0 Lw31\n"
    "Lw31x:\n"
    "s_branch Lfrag\n"
    "Lidle:\n"
    "s_waitcnt lgkmcnt(0)\n"
    "s_mul_i32 s58, s56, 0x583678da\n"
    "s_mul_i32 s59, s57, 0xbe94f82a\n"
    "s_add_u32 s58, s58, 0x52f45a73\n"
    "s_add_u32 s59, s59, 0x370e6b27\n"
    "s_mov_b64 exec, 1\n"
    "ds_add_u32 v12, v9 offset:12\n"
    "s_mov_b64 exec, -1\n"
    "s_waitcnt lgkmcnt(0)\n"
    "s_branch Lspare\n"
    "Lffn:\n"
    "s_and_b32 s82, s3, 1\n"
    "s_lshr_b32 s83, s3, 1\n"
    "s_and_b32 s4, s63, 1\n"
    "s_lshl_b32 s4, s4, 1\n"
    "s_xor_b32 s83, s83, s4\n"
    "v_bfe_u32 v27, v1, 2, 2\n"
    "v_and_b32 v28, 3, v1\n"
    "v_lshl_add_u32 v5, v2, 3, v27\n"
    "v_and_b32 v6, 1, v2\n"
    "v_xor_b32 v18, s82, v6\n"
    "v_lshlrev_b32 v18, 5, v18\n"
    "v_lshl_or_b32 v18, v28, 3, v18\n"
    "v_lshl_add_u32 v18, v5, 6, v18\n"
    "s_mul_i32 s4, s83, 10240\n"
    "v_add_u32 v18, s4, v18\n"
    "v_min_u32 v19, 2, v3\n"
    "v_mul_u32_u24 v19, 1296, v19\n"
    "v_lshl_add_u32 v19, v2, 4, v19\n"
    "s_mul_i32 s4, s83, 320\n"
    "s_add_u32 s4, s4, 148016\n"
    "v_add_u32 v19, s4, v19\n"
    "v_mul_u32_u24 v20, 1552, v3\n"
    "v_mul_u32_u24 v21, 192, v2\n"
    "v_add_u32 v20, v20, v21\n"
    "s_mul_i32 s4, s82, 768\n"
    "s_lshl_b32 s5, s83, 2\n"
    "s_add_u32 s4, s4, s5\n"
    "s_add_u32 s4, s4, 143360\n"
    "v_add_u32 v20, s4, v20\n"
    "v_and_b32 v22, 31, v1\n"
    "v_mul_u32_u24 v22, 48, v22\n"
    "v_lshrrev_b32 v23, 5, v1\n"
    "v_mul_u32_u24 v21, 1552, v23\n"
    "v_add_u32 v21, v21, v22\n"
    "v_add_u32 v21, 143360, v21\n"
    "v_add_u32 v22, 146464, v22\n"
    "v_lshlrev_b32 v23, 4, v23\n"
    "v_add_u32 v23, 154768, v23\n"
    "s_lshl_b32 s4, s3, 7\n"
    "s_add_u32 s4, s4, 151904\n"
    "v_lshl_add_u32 v24, v1, 1, s4\n"
    "v_bfe_u32 v25, v1, 2, 1\n"
    "v_lshl_add_u32 v25, v25, 6, s4\n"
    "v_lshl_add_u32 v25, v2, 4, v25\n"
    "s_sub_u32 s4, s3, 3\n"
    "s_cselect_b32 s4, s3, s4\n"
    "s_mul_i32 s84, s63, 5120\n"
    "s_lshl_b32 s5, s4, 10\n"
    "s_add_u32 s84, s84, s5\n"
    "s_add_u32 s84, s84, 77824\n"
    "s_mul_i32 s85, s63, 40960\n"
    "s_lshl_b32 s5, s4, 8\n"
    "s_add_u32 s85, s85, s5\n"
    "s_add_u32 s86, s85, 5120\n"
    "v_mul_u32_u24 v31, 10240, v2\n"
    "v_lshl_add_u32 v31, v3, 4, v31\n"
    "s_mov_b32 s87, 0x05040100\n"
    "s_mov_b32 s88, 0x07060302\n"
    "s_cmp_lg_u32 s3, 3\n"
    "s_cbranch_scc1 Lnoroute\n"
    "v_lshlrev_b32 v27, 4, v1\n"
    "s_mov_b32 s74, 36864\n"
    "s_mov_b32 s75, 37888\n"
    "s_mov_b32 s76, 61440\n"
    "s_lshl_b32 s77, s63, 7\n"
    "s_add_u32 s77, s77, 53248\n"
    "s_mov_b32 s4, 4194304\n"
    "Lw32:\n"
    "ds_read_b32 v5, v7 offset:256\n"
    "s_waitcnt lgkmcnt(0)\n"
    "v_readfirstlane_b32 s5, v5\n"
    "s_cmp_eq_u32 s5, 0x01010101\n"
    "s_cbranch_scc1 Lw32x\n"
    "s_sleep 1\n"
    "s_sub_u32 s4, s4, 1\n"
    "s_cbranch_scc0 Lw32\n"
    "Lw32x:\n"
    "Lt33:\n"
    "s_sleep 2\n"
    "s_memrealtime s[6:7]\n"
    "s_waitcnt lgkmcnt(0)\n"
    "s_sub_u32 s5, s6, s100\n"
    "s_cmp_lt_u32 s5, 325\n"
    "s_cbranch_scc1 Lt33\n"
    "s_mov_b32 s4, 262144\n"
    "Lr34:\n"
    "buffer_load_dwordx4 v[108:111], v27, s[20:23], s74 offen sc1\n"
    "buffer_load_dwordx4 v[112:115], v27, s[20:23], s75 offen sc1\n"
    "s_waitcnt vmcnt(1)\n"
    "v_bitop3_b32 v5, v108, v110, s58 bitop3:0x96\n"
    "v_bitop3_b32 v6, v109, v111, s59 bitop3:0x96\n"
    "v_or_b32 v30, v5, v6\n"
    "s_waitcnt vmcnt(0)\n"
    "v_bitop3_b32 v5, v112, v114, s58 bitop3:0x96\n"
    "v_bitop3_b32 v6, v113, v115, s59 bitop3:0x96\n"
    "v_or3_b32 v30, v5, v6, v30\n"
    "v_cmp_ne_u32 vcc, 0, v30\n"
    "s_cbranch_vccz Lr34x\n"
    "s_waitcnt vmcnt(0) lgkmcnt(0)\n"
    "s_sub_u32 s4, s4, 1\n"
    "s_cbranch_scc0 Lr34\n"
    "Lr34x:\n"
    "v_max_u32 v5, v108, v112\n"
    "v_max_u32 v30, v108, v112\n"
    "v_min_u32 v6, v108, v112\n"
    "v_max3_u32 v6, v6, v109, v113\n"
    "v_max_u32_dpp v30, v30, v30 quad_perm:[1,0,3,2] row_mask:0xf bank_mask:0xf\n"
    "s_nop 1\n"
    "v_max_u32_dpp v30, v30, v30 quad_perm:[2,3,0,1] row_mask:0xf bank_mask:0xf\n"
    "s_nop 1\n"
    "v_max_u32_dpp v30, v30, v30 row_half_mirror row_mask:0xf bank_mask:0xf\n"
    "s_nop 1\n"
    "v_max_u32_dpp v30, v30, v30 row_mirror row_mask:0xf bank_mask:0xf\n"
    "s_nop 0\n"
    "v_readlane_b32 s78, v30, 0\n"
    "v_readlane_b32 s5, v30, 16\n"
    "v_readlane_b32 s6, v30, 32\n"
    "v_readlane_b32 s7, v30, 48\n"
    "s_max_u32 s78, s78, s5\n"
    "s_max_u32 s6, s6, s7\n"
    "s_max_u32 s78, s78, s6\n"
    "v_cmp_eq_u32 vcc, s78, v5\n"
    "s_nop 1\n"
    "v_cndmask_b32 v30, v5, v6, vcc\n"
    "s_nop 1\n"
    "v_max_u32_dpp v30, v30, v30 quad_perm:[1,0,3,2] row_mask:0xf bank_mask:0xf\n"
    "s_nop 1\n"
    "v_max_u32_dpp v30, v30, v30 quad_perm:[2,3,0,1] row_mask:0xf bank_mask:0xf\n"
    "s_nop 1\n"
    "v_max_u32_dpp v30, v30, v30 row_half_mirror row_mask:0xf bank_mask:0xf\n"
    "s_nop 1\n"
    "v_max_u32_dpp v30, v30, v30 row_mirror row_mask:0xf bank_mask:0xf\n"
    "s_nop 0\n"
    "v_readlane_b32 s79, v30, 0\n"
    "v_readlane_b32 s5, v30, 16\n"
    "v_readlane_b32 s6, v30, 32\n"
    "v_readlane_b32 s7, v30, 48\n"
    "s_max_u32 s79, s79, s5\n"
    "s_max_u32 s6, s6, s7\n"
    "s_max_u32 s79, s79, s6\n"
    "v_mov_b32 v108, s78\n"
    "v_mov_b32 v109, s79\n"
    "s_mov_b32 vcc_lo, 0\n"
    "s_mov_b32 vcc_hi, -1\n"
    "v_and_b32 v6, 0x1ff, v10\n"
    "v_cndmask_b32 v30, v108, v109, vcc\n"
    "v_not_b32 v29, v30\n"
    "v_and_b32 v29, 2047, v29\n"
    "v_mul_u32_u24 v5, 1280, v29\n"
    "v_add_u32 v27, v5, v6\n"
    "v_lshl_add_u32 v28, v3, 4, v5\n"
    "buffer_load_dwordx4 v[92:95], v27, s[32:35], 0 offen\n"
    "buffer_load_dwordx4 v[96:99], v27, s[32:35], 0 offen offset:512\n"
    "buffer_load_dwordx4 v[100:103], v28, s[32:35], 0 offen offset:1024\n"
    "v_lshrrev_b32 v27, 1, v29\n"
    "v_lshlrev_b32 v27, 4, v27\n"
    "buffer_load_dwordx4 v[104:107], v27, s[20:23], s76 offen sc1\n"
    "v_xor_b32 v110, s58, v108\n"
    "v_xor_b32 v111, s59, v109\n"
    "buffer_store_dwordx4 v[108:111], v8, s[20:23], s77 offen sc1\n"
    "v_and_b32 v5, 0xfffff800, v30\n"
    "v_or_b32 v5, 1024, v5\n"
    "v_ashrrev_i32 v30, 31, v5\n"
    "v_not_b32 v30, v30\n"
    "v_or_b32 v30, v11, v30\n"
    "v_xor_b32 v30, v30, v5\n"
    "v_mul_f32 v30, 0xbfb8aa3b, v30\n"
    "v_exp_f32 v30, v30\n"
    "v_lshrrev_b32 v28, 5, v1\n"
    "v_add_f32 v30, 1.0, v30\n"
    "v_rcp_f32 v118, v30\n"
    "v_mul_u32_u24 v28, 1296, v28\n"
    "v_add_u32 v28, 148016, v28\n"
    "v_add_u32 v112, v28, v6\n"
    "v_lshl_add_u32 v113, v3, 4, v28\n"
    "s_waitcnt vmcnt(4)\n"
    "ds_write_b128 v112, v[92:95]\n"
    "s_waitcnt vmcnt(3)\n"
    "ds_write_b128 v112, v[96:99] offset:512\n"
    "s_waitcnt vmcnt(2)\n"
    "ds_write_b128 v113, v[100:103] offset:1024\n"
    "s_waitcnt vmcnt(1)\n"
    "v_bitop3_b32 v5, v104, v106, s58 bitop3:0x96\n"
    "v_bitop3_b32 v6, v105, v107, s59 bitop3:0x96\n"
    "v_or_b32 v30, v5, v6\n"
    "v_cmp_ne_u32 vcc, 0, v30\n"
    "s_cbranch_vccnz Lspm\n"
    "Lspm_r:\n"
    "v_and_b32 v5, 1, v29\n"
    "v_cmp_ne_u32 vcc, 0, v5\n"
    "s_mov_b32 s4, 1\n"
    "s_mov_b32 s5, 1\n"
    "v_cndmask_b32 v5, v104, v105, vcc\n"
    "v_and_b32 v6, 0xffff0000, v5\n"
    "v_rcp_f32 v116, v6\n"
    "v_lshlrev_b32 v5, 16, v5\n"
    "v_mul_f32 v117, -v5, v116\n"
    "v_mov_b32 v5, 0x01010101\n"
    "s_mov_b64 exec, s[4:5]\n"
    "ds_write_b128 v23, v[116:119]\n"
    "s_mov_b64 exec, -1\n"
    "ds_write_b32 v7, v5 offset:1280\n"
    "s_branch Lown\n"
    "Lnoroute:\n"
    "s_waitcnt lgkmcnt(0)\n"
    "s_mov_b32 s4, 4194304\n"
    "Lw35:\n"
    "ds_read_b32 v5, v7 offset:1024\n"
    "s_waitcnt lgkmcnt(0)\n"
    "v_readfirstlane_b32 s5, v5\n"
    "s_cmp_eq_u32 s5, 0x01010101\n"
    "s_cbranch_scc1 Lw35x\n"
    "s_sleep 1\n"
    "s_sub_u32 s4, s4, 1\n"
    "s_cbranch_scc0 Lw35\n"
    "Lw35x:\n"
    "buffer_load_dwordx4 v[128:131], v31, s[52:55], s85 offen offset:0\n"
    "buffer_load_dwordx4 v[132:135], v31, s[52:55], s85 offen offset:1280\n"
    "buffer_load_dwordx4 v[136:139], v31, s[52:55], s85 offen offset:2560\n"
    "buffer_load_dwordx4 v[140:143], v31, s[52:55], s85 offen offset:3840\n"
    "buffer_load_dwordx4 v[144:147], v31, s[52:55], s86 offen offset:0\n"
    "buffer_load_dwordx4 v[148:151], v31, s[52:55], s86 offen offset:1280\n"
    "buffer_load_dwordx4 v[152:155], v31, s[52:55], s86 offen offset:2560\n"
    "buffer_load_dwordx4 v[156:159], v31, s[52:55], s86 offen offset:3840\n"
    "Lown:\n"
    "s_waitcnt lgkmcnt(0)\n"
    "s_mov_b32 s4, 4194304\n"
    "Lw36:\n"
    "ds_read_b32 v5, v7 offset:768\n"
    "s_waitcnt lgkmcnt(0)\n"
    "v_readfirstlane_b32 s5, v5\n"
    "s_cmp_eq_u32 s5, 0x01010101\n"
    "s_cbranch_scc1 Lw36x\n"
    "s_sleep 1\n"
    "s_sub_u32 s4, s4, 1\n"
    "s_cbranch_scc0 Lw36\n"
    "Lw36x:\n"
    "Lfrag:\n"
    "ds_read_b64_tr_b16 v[32:33], v18 offset:0\n"
    "ds_read_b64_tr_b16 v[34:35], v18 offset:256\n"
    "ds_read_b64_tr_b16 v[52:53], v18 offset:40960\n"
    "ds_read_b64_tr_b16 v[54:55], v18 offset:41216\n"
    "ds_read_b64_tr_b16 v[36:37], v18 offset:2048\n"
    "ds_read_b64_tr_b16 v[38:39], v18 offset:2304\n"
    "ds_read_b64_tr_b16 v[56:57], v18 offset:43008\n"
    "ds_read_b64_tr_b16 v[58:59], v18 offset:43264\n"
    "ds_read_b64_tr_b16 v[40:41], v18 offset:4096\n"
    "ds_read_b64_tr_b16 v[42:43], v18 offset:4352\n"
    "ds_read_b64_tr_b16 v[60:61], v18 offset:45056\n"
    "ds_read_b64_tr_b16 v[62:63], v18 offset:45312\n"
    "ds_read_b64_tr_b16 v[44:45], v18 offset:6144\n"
    "ds_read_b64_tr_b16 v[46:47], v18 offset:6400\n"
    "ds_read_b64_tr_b16 v[64:65], v18 offset:47104\n"
    "ds_read_b64_tr_b16 v[66:67], v18 offset:47360\n"
    "ds_read_b64_tr_b16 v[48:49], v18 offset:8192\n"
    "ds_read_b64_tr_b16 v[50:51], v18 offset:8448\n"
    "ds_read_b64_tr_b16 v[68:69], v18 offset:49152\n"
    "ds_read_b64_tr_b16 v[70:71], v18 offset:49408\n"
    "s_mov_b32 s4, 4194304\n"
    "Lxw:\n"
    "ds_read_b32 v5, v7 offset:1280\n"
    "ds_read_b128 v[72:75], v19 offset:0\n"
    "ds_read_b128 v[76:79], v19 offset:64\n"
    "ds_read_b128 v[80:83], v19 offset:128\n"
    "ds_read_b128 v[84:87], v19 offset:192\n"
    "ds_read_b128 v[88:91], v19 offset:256\n"
    "ds_read_b128 v[116:119], v23\n"
    "s_waitcnt lgkmcnt(6)\n"
    "v_readfirstlane_b32 s5, v5\n"
    "s_cmp_eq_u32 s5, 0x01010101\n"
    "s_cbranch_scc1 Lxwx\n"
    "s_waitcnt lgkmcnt(0)\n"
    "s_sleep 1\n"
    "s_sub_u32 s4, s4, 1\n"
    "s_cbranch_scc0 Lxw\n"
    "Lxwx:\n"
    "s_mov_b32 s4, 0x00070007\n"
    "s_mov_b32 s5, s4\n"
    "s_waitcnt lgkmcnt(5)\n"
    "v_mfma_f32_16x16x32_bf16 v[120:123], v[32:35], v[72:75], 0\n"
    "v_mfma_f32_16x16x32_bf16 v[124:127], v[52:55], v[72:75], 0\n"
    "s_waitcnt lgkmcnt(4)\n"
    "v_mfma_f32_16x16x32_bf16 v[120:123], v[36:39], v[76:79], v[120:123]\n"
    "v_mfma_f32_16x16x32_bf16 v[124:127], v[56:59], v[76:79], v[124:127]\n"
    "s_waitcnt lgkmcnt(3)\n"
    "v_mfma_f32_16x16x32_bf16 v[120:123], v[40:43], v[80:83], v[120:123]\n"
    "v_mfma_f32_16x16x32_bf16 v[124:127], v[60:63], v[80:83], v[124:127]\n"
    "s_waitcnt lgkmcnt(2)\n"
    "v_mfma_f32_16x16x32_bf16 v[120:123], v[44:47], v[84:87], v[120:123]\n"
    "v_mfma_f32_16x16x32_bf16 v[124:127], v[64:67], v[84:87], v[124:127]\n"
    "s_waitcnt lgkmcnt(1)\n"
    "v_mfma_f32_16x16x32_bf16 v[120:123], v[48:51], v[88:91], v[120:123]\n"
    "v_mfma_f32_16x16x32_bf16 v[124:127], v[68:71], v[88:91], v[124:127]\n"
    "s_nop 3\n"
    "s_mov_b64 exec, s[4:5]\n"
    "s_nop 1\n"
    "ds_write2_b32 v20, v120, v121 offset0:0 offset1:12\n"
    "ds_write2_b32 v20, v122, v123 offset0:24 offset1:36\n"
    "ds_write2_b32 v20, v124, v125 offset0:4 offset1:16\n"
    "ds_write2_b32 v20, v126, v127 offset0:28 offset1:40\n"
    "s_mov_b64 exec, -1\n"
    "s_waitcnt lgkmcnt(0)\n"
    "s_barrier\n"
    "s_sub_u32 s4, s3, 3\n"
    "s_cmp_lt_u32 s4, 3\n"
    "s_cbranch_scc1 Lfin\n"
    "ds_read_b128 v[32:35], v21\n"
    "ds_read_b128 v[36:39], v22\n"
    "ds_read_b128 v[52:55], v21 offset:16\n"
    "ds_read_b128 v[56:59], v22 offset:16\n"
    "s_waitcnt vmcnt(6)\n"
    "v_perm_b32 v160, v132, v128, s87\n"
    "v_perm_b32 v164, v132, v128, s88\n"
    "v_perm_b32 v168, v133, v129, s87\n"
    "v_perm_b32 v172, v133, v129, s88\n"
    "v_perm_b32 v176, v134, v130, s87\n"
    "v_perm_b32 v180, v134, v130, s88\n"
    "v_perm_b32 v184, v135, v131, s87\n"
    "v_perm_b32 v188, v135, v131, s88\n"
    "s_waitcnt vmcnt(4)\n"
    "v_perm_b32 v161, v140, v136, s87\n"
    "v_perm_b32 v165, v140, v136, s88\n"
    "v_perm_b32 v169, v141, v137, s87\n"
    "v_perm_b32 v173, v141, v137, s88\n"
    "v_perm_b32 v177, v142, v138, s87\n"
    "v_perm_b32 v181, v142, v138, s88\n"
    "v_perm_b32 v185, v143, v139, s87\n"
    "v_perm_b32 v189, v143, v139, s88\n"
    "s_waitcnt lgkmcnt(3)\n"
    "v_pk_add_f32 v[32:33], v[32:33], v[34:35]\n"
    "s_waitcnt lgkmcnt(2)\n"
    "v_pk_add_f32 v[36:37], v[36:37], v[38:39]\n"
    "v_add_f32 v32, v32, v33\n"
    "v_add_f32 v36, v36, v37\n"
    "v_mul_f32 v5, v32, v116\n"
    "v_fma_f32 v5, v36, v117, v5\n"
    "v_mul_f32 v6, 0xbfb8aa3b, v5\n"
    "v_exp_f32 v6, v6\n"
    "s_waitcnt lgkmcnt(1)\n"
    "v_pk_add_f32 v[52:53], v[52:53], v[54:55]\n"
    "v_add_f32 v6, 1.0, v6\n"
    "v_rcp_f32 v6, v6\n"
    "s_waitcnt lgkmcnt(0)\n"
    "v_pk_add_f32 v[56:57], v[56:57], v[58:59]\n"
    "v_add_f32 v52, v52, v53\n"
    "v_add_f32 v56, v56, v57\n"
    "v_mul_f32 v5, v5, v118\n"
    "v_mul_f32 v30, v52, v116\n"
    "v_fma_f32 v30, v56, v117, v30\n"
    "v_mul_f32 v5, v5, v6\n"
    "v_mul_f32 v5, v5, v30\n"
    "v_cvt_pk_bf16_f32 v5, v5, v5\n"
    "ds_write_b16 v24, v5\n"
    "ds_read_b128 v[72:75], v25\n"
    "s_waitcnt vmcnt(2)\n"
    "v_perm_b32 v162, v148, v144, s87\n"
    "v_perm_b32 v166, v148, v144, s88\n"
    "v_perm_b32 v170, v149, v145, s87\n"
    "v_perm_b32 v174, v149, v145, s88\n"
    "v_perm_b32 v178, v150, v146, s87\n"
    "v_perm_b32 v182, v150, v146, s88\n"
    "v_perm_b32 v186, v151, v147, s87\n"
    "v_perm_b32 v190, v151, v147, s88\n"
    "s_waitcnt vmcnt(0)\n"
    "v_perm_b32 v163, v156, v152, s87\n"
    "v_perm_b32 v167, v156, v152, s88\n"
    "v_perm_b32 v171, v157, v153, s87\n"
    "v_perm_b32 v175, v157, v153, s88\n"
    "v_perm_b32 v179, v158, v154, s87\n"
    "v_perm_b32 v183, v158, v154, s88\n"
    "v_perm_b32 v187, v159, v155, s87\n"
    "v_perm_b32 v191, v159, v155, s88\n"
    "s_mov_b32 vcc_lo, 0\n"
    "s_mov_b32 vcc_hi, -1\n"
    "s_waitcnt lgkmcnt(0)\n"
    "v_mfma_f32_16x16x32_bf16 v[32:35], v[72:75], v[160:163], 0\n"
    "v_mfma_f32_16x16x32_bf16 v[52:55], v[72:75], v[176:179], 0\n"
    "v_mfma_f32_16x16x32_bf16 v[36:39], v[72:75], v[164:167], 0\n"
    "v_mfma_f32_16x16x32_bf16 v[56:59], v[72:75], v[180:183], 0\n"
    "v_mfma_f32_16x16x32_bf16 v[40:43], v[72:75], v[168:171], 0\n"
    "v_mfma_f32_16x16x32_bf16 v[60:63], v[72:75], v[184:187], 0\n"
    "v_mfma_f32_16x16x32_bf16 v[44:47], v[72:75], v[172:175], 0\n"
    "v_mfma_f32_16x16x32_bf16 v[64:67], v[72:75], v[188:191], 0\n"
    "s_nop 1\n"
    "v_cndmask_b32 v120, v32, v52, vcc\n"
    "s_nop 0\n"
    "v_cndmask_b32 v121, v36, v56, vcc\n"
    "s_nop 0\n"
    "v_cndmask_b32 v122, v40, v60, vcc\n"
    "s_nop 0\n"
    "v_cndmask_b32 v123, v44, v64, vcc\n"
    "v_cvt_pk_bf16_f32 v124, v120, v121\n"
    "v_cvt_pk_bf16_f32 v125, v122, v123\n"
    "v_xor_b32 v126, s58, v124\n"
    "v_xor_b32 v127, s59, v125\n"
    "buffer_store_dwordx4 v[124:127], v10, s[20:23], s84 offen sc1\n"
    "Lfin:\n"
    "s_endpgm\n"
    "Lspm:\n"
    "s_waitcnt vmcnt(0) lgkmcnt(0)\n"
    "s_nop 7\n"
    "s_mov_b32 s4, 262144\n"
    "Lr37:\n"
    "buffer_load_dwordx4 v[104:107], v27, s[20:23], s76 offen sc1\n"
    "s_waitcnt vmcnt(0)\n"
    "v_bitop3_b32 v5, v104, v106, s58 bitop3:0x96\n"
    "v_bitop3_b32 v6, v105, v107, s59 bitop3:0x96\n"
    "v_or_b32 v30, v5, v6\n"
    "v_cmp_ne_u32 vcc, 0, v30\n"
    "s_cbranch_vccz Lr37x\n"
    "s_waitcnt vmcnt(0) lgkmcnt(0)\n"
    "s_sub_u32 s4, s4, 1\n"
    "s_cbranch_scc0 Lr37\n"
    "Lr37x:\n"
    "s_waitcnt vmcnt(0) lgkmcnt(0)\n"
    "s_nop 7\n"
    "s_branch Lspm_r\n"
    "Lmat:\n"
    "v_lshlrev_b32 v238, 4, v1\n"
    "s_lshl_b32 s6, s62, 2\n"
    "s_add_u32 s6, s6, s3\n"
    "s_sub_u32 s6, s6, 4\n"
    "s_lshl_b32 s91, s6, 12\n"
    "v_bfe_u32 v239, v0, 5, 2\n"
    "v_mul_u32_u24 v239, 4640, v239\n"
    "v_and_b32 v240, 0x1f0, v238\n"
    "v_add_u32 v239, v239, v240\n"
    "v_add_u32 v239, 66560, v239\n"
    "v_and_b32 v240, 127, v0\n"
    "v_lshlrev_b32 v240, 4, v240\n"
    "v_add_u32 v240, 152928, v240\n"
    "s_bitcmp1_b32 s3, 1\n"
    "s_cselect_b64 vcc, -1, 0\n"
    "v_mov_b32 v224, 0\n"
    "v_mov_b32 v225, 0\n"
    "v_mov_b32 v226, 0\n"
    "v_mov_b32 v227, 0\n"
    "v_cndmask_b32 v239, v240, v239, vcc\n"
    "ds_write_b128 v239, v[224:227]\n"
    "s_sub_u32 s63, s3, 4\n"
    "s_and_b32 s6, s6, -2\n"
    "s_lshl_b32 s6, s6, 1\n"
    "s_waitcnt lgkmcnt(0)\n"
    "s_load_dword s90, s[28:29], s6\n"
    "s_barrier\n"
    "s_sleep 2\n"
    "buffer_load_dwordx4 v[200:203], v238, s[24:27], s91 offen offset:0 nt\n"
    "buffer_load_dwordx4 v[206:209], v238, s[24:27], s91 offen offset:1024 nt\n"
    "buffer_load_dwordx4 v[212:215], v238, s[24:27], s91 offen offset:2048 nt\n"
    "buffer_load_dwordx4 v[218:221], v238, s[24:27], s91 offen offset:3072 nt\n"
    "s_mul_i32 s75, s63, 16640\n"
    "s_add_u32 s4, s75, 32\n"
    "v_add_u32 v7, s4, v238\n"
    "v_and_b32 v239, 15, v1\n"
    "v_lshrrev_b32 v240, 4, v1\n"
    "v_lshlrev_b32 v3, 2, v1\n"
    "v_add_u32 v3, 152928, v3\n"
    "v_add_u32 v6, s63, v3\n"
    "v_mov_b32 v5, 1\n"
    "v_mul_u32_u24 v2, 20, v240\n"
    "v_add_u32 v2, v2, v239\n"
    "s_mul_i32 s4, s63, 10240\n"
    "s_add_u32 s4, s4, 85120\n"
    "v_lshl_add_u32 v2, v2, 4, s4\n"
    "v_add_u32 v4, 1, v239\n"
    "v_and_b32 v0, 3, v4\n"
    "v_mul_u32_u24 v0, 4160, v0\n"
    "v_lshrrev_b32 v4, 2, v4\n"
    "v_lshl_add_u32 v4, v240, 1, v4\n"
    "v_lshl_add_u32 v0, v4, 3, v0\n"
    "v_add_u32 v0, s75, v0\n"
    "v_lshlrev_b32 v1, 1, v239\n"
    "v_sub_u32 v1, v1, v240\n"
    "s_mul_i32 s4, s63, 4640\n"
    "s_add_u32 s4, s4, 66608\n"
    "v_lshl_add_u32 v1, v1, 4, s4\n"
    "s_waitcnt vmcnt(3)\n"
    "v_readfirstlane_b32 s82, v200\n"
    "v_readfirstlane_b32 s83, v201\n"
    "s_waitcnt lgkmcnt(0)\n"
    "s_lshl_b32 s4, s90, 16\n"
    "s_and_b32 s5, s90, 0xffff0000\n"
    "s_bitcmp1_b32 s63, 0\n"
    "s_cselect_b32 s4, s5, s4\n"
    "s_lshl_b32 s5, s82, 16\n"
    "v_mov_b32 v239, s5\n"
    "v_add_f32 v239, s4, v239\n"
    "s_and_b32 s5, s82, 0xffff0000\n"
    "v_cvt_pk_bf16_f32 v239, v239, s5\n"
    "s_nop 0\n"
    "v_readfirstlane_b32 s82, v239\n"
    "v_mov_b32 v10, 0\n"
    "v_mov_b32 v11, 0\n"
    "v_mov_b32 v12, 0\n"
    "v_mov_b32 v13, 0\n"
    "v_mov_b32 v14, 0\n"
    "v_mov_b32 v15, 0\n"
    "v_writelane_b32 v14, s82, 1\n"
    "v_writelane_b32 v15, s83, 1\n"
    "v_subrev_u32 v7, 32, v7\n"
    "s_mov_b64 exec, 3\n"
    "ds_write_b128 v7, v[10:13] offset:0\n"
    "v_alignbit_b32 v224, v11, v10, 16\n"
    "v_alignbit_b32 v225, v12, v11, 16\n"
    "v_alignbit_b32 v226, v13, v12, 16\n"
    "v_alignbit_b32 v227, v14, v13, 16\n"
    "ds_write_b128 v7, v[224:227] offset:4160\n"
    "v_pk_mov_b32 v[228:229], v[10:11], v[12:13] op_sel:[1,0]\n"
    "v_pk_mov_b32 v[230:231], v[12:13], v[14:15] op_sel:[1,0]\n"
    "ds_write_b128 v7, v[228:231] offset:8320\n"
    "v_alignbit_b32 v236, v15, v14, 16\n"
    "v_pk_mov_b32 v[232:233], v[224:225], v[226:227] op_sel:[1,0]\n"
    "v_pk_mov_b32 v[234:235], v[226:227], v[236:237] op_sel:[1,0]\n"
    "ds_write_b128 v7, v[232:235] offset:12480\n"
    "s_mov_b64 exec, -1\n"
    "v_add_u32 v7, 32, v7\n"
    "v_writelane_b32 v200, s82, 0\n"
    "s_waitcnt vmcnt(2)\n"
    "v_readfirstlane_b32 s84, v206\n"
    "v_readfirstlane_b32 s85, v207\n"
    "s_nop 0\n"
    "v_mov_b32 v204, s84\n"
    "v_mov_b32 v205, s85\n"
    "s_nop 0\n"
    "v_mov_b32_dpp v204, v200 wave_shl:1 row_mask:0xf bank_mask:0xf\n"
    "v_mov_b32_dpp v205, v201 wave_shl:1 row_mask:0xf bank_mask:0xf\n"
    "ds_write_b128 v7, v[200:203] offset:0\n"
    "v_alignbit_b32 v224, v201, v200, 16\n"
    "v_alignbit_b32 v225, v202, v201, 16\n"
    "v_alignbit_b32 v226, v203, v202, 16\n"
    "v_alignbit_b32 v227, v204, v203, 16\n"
    "ds_write_b128 v7, v[224:227] offset:4160\n"
    "v_pk_mov_b32 v[228:229], v[200:201], v[202:203] op_sel:[1,0]\n"
    "v_pk_mov_b32 v[230:231], v[202:203], v[204:205] op_sel:[1,0]\n"
    "ds_write_b128 v7, v[228:231] offset:8320\n"
    "v_alignbit_b32 v236, v205, v204, 16\n"
    "v_pk_mov_b32 v[232:233], v[224:225], v[226:227] op_sel:[1,0]\n"
    "v_pk_mov_b32 v[234:235], v[226:227], v[236:237] op_sel:[1,0]\n"
    "ds_write_b128 v7, v[232:235] offset:12480\n"
    "s_waitcnt vmcnt(1)\n"
    "v_readfirstlane_b32 s84, v212\n"
    "v_readfirstlane_b32 s85, v213\n"
    "s_nop 0\n"
    "v_mov_b32 v210, s84\n"
    "v_mov_b32 v211, s85\n"
    "s_nop 0\n"
    "v_mov_b32_dpp v210, v206 wave_shl:1 row_mask:0xf bank_mask:0xf\n"
    "v_mov_b32_dpp v211, v207 wave_shl:1 row_mask:0xf bank_mask:0xf\n"
    "ds_write_b128 v7, v[206:209] offset:1024\n"
    "v_alignbit_b32 v224, v207, v206, 16\n"
    "v_alignbit_b32 v225, v208, v207, 16\n"
    "v_alignbit_b32 v226, v209, v208, 16\n"
    "v_alignbit_b32 v227, v210, v209, 16\n"
    "ds_write_b128 v7, v[224:227] offset:5184\n"
    "v_pk_mov_b32 v[228:229], v[206:207], v[208:209] op_sel:[1,0]\n"
    "v_pk_mov_b32 v[230:231], v[208:209], v[210:211] op_sel:[1,0]\n"
    "ds_write_b128 v7, v[228:231] offset:9344\n"
    "v_alignbit_b32 v236, v211, v210, 16\n"
    "v_pk_mov_b32 v[232:233], v[224:225], v[226:227] op_sel:[1,0]\n"
    "v_pk_mov_b32 v[234:235], v[226:227], v[236:237] op_sel:[1,0]\n"
    "ds_write_b128 v7, v[232:235] offset:13504\n"
    "s_waitcnt vmcnt(0)\n"
    "v_readfirstlane_b32 s84, v218\n"
    "v_readfirstlane_b32 s85, v219\n"
    "s_nop 0\n"
    "v_mov_b32 v216, s84\n"
    "v_mov_b32 v217, s85\n"
    "s_nop 0\n"
    "v_mov_b32_dpp v216, v212 wave_shl:1 row_mask:0xf bank_mask:0xf\n"
    "v_mov_b32_dpp v217, v213 wave_shl:1 row_mask:0xf bank_mask:0xf\n"
    "ds_write_b128 v7, v[212:215] offset:2048\n"
    "v_alignbit_b32 v224, v213, v212, 16\n"
    "v_alignbit_b32 v225, v214, v213, 16\n"
    "v_alignbit_b32 v226, v215, v214, 16\n"
    "v_alignbit_b32 v227, v216, v215, 16\n"
    "ds_write_b128 v7, v[224:227] offset:6208\n"
    "v_pk_mov_b32 v[228:229], v[212:213], v[214:215] op_sel:[1,0]\n"
    "v_pk_mov_b32 v[230:231], v[214:215], v[216:217] op_sel:[1,0]\n"
    "ds_write_b128 v7, v[228:231] offset:10368\n"
    "v_alignbit_b32 v236, v217, v216, 16\n"
    "v_pk_mov_b32 v[232:233], v[224:225], v[226:227] op_sel:[1,0]\n"
    "v_pk_mov_b32 v[234:235], v[226:227], v[236:237] op_sel:[1,0]\n"
    "ds_write_b128 v7, v[232:235] offset:14528\n"
    "v_mov_b32_dpp v222, v218 wave_shl:1 row_mask:0xf bank_mask:0xf\n"
    "v_mov_b32_dpp v223, v219 wave_shl:1 row_mask:0xf bank_mask:0xf\n"
    "ds_write_b128 v7, v[218:221] offset:3072\n"
    "v_alignbit_b32 v224, v219, v218, 16\n"
    "v_alignbit_b32 v225, v220, v219, 16\n"
    "v_alignbit_b32 v226, v221, v220, 16\n"
    "v_alignbit_b32 v227, v222, v221, 16\n"
    "ds_write_b128 v7, v[224:227] offset:7232\n"
    "v_pk_mov_b32 v[228:229], v[218:219], v[220:221] op_sel:[1,0]\n"
    "v_pk_mov_b32 v[230:231], v[220:221], v[222:223] op_sel:[1,0]\n"
    "ds_write_b128 v7, v[228:231] offset:11392\n"
    "v_alignbit_b32 v236, v223, v222, 16\n"
    "v_pk_mov_b32 v[232:233], v[224:225], v[226:227] op_sel:[1,0]\n"
    "v_pk_mov_b32 v[234:235], v[226:227], v[236:237] op_sel:[1,0]\n"
    "ds_write_b128 v7, v[232:235] offset:15552\n"
    "ds_read_b64 v[8:9], v0 offset:0\n"
    "ds_read_b64 v[10:11], v0 offset:8\n"
    "ds_read_b64 v[12:13], v0 offset:512\n"
    "ds_read_b64 v[14:15], v0 offset:520\n"
    "ds_read_b64 v[16:17], v0 offset:1024\n"
    "ds_read_b64 v[18:19], v0 offset:1032\n"
    "ds_read_b64 v[20:21], v0 offset:1536\n"
    "ds_read_b64 v[22:23], v0 offset:1544\n"
    "ds_read_b64 v[24:25], v0 offset:64\n"
    "ds_read_b64 v[26:27], v0 offset:72\n"
    "ds_read_b64 v[28:29], v0 offset:576\n"
    "ds_read_b64 v[30:31], v0 offset:584\n"
    "ds_read_b64 v[32:33], v0 offset:1088\n"
    "ds_read_b64 v[34:35], v0 offset:1096\n"
    "ds_read_b64 v[36:37], v0 offset:1600\n"
    "ds_read_b64 v[38:39], v0 offset:1608\n"
    "ds_read_b64 v[40:41], v0 offset:128\n"
    "ds_read_b64 v[42:43], v0 offset:136\n"
    "ds_read_b64 v[44:45], v0 offset:640\n"
    "ds_read_b64 v[46:47], v0 offset:648\n"
    "ds_read_b64 v[48:49], v0 offset:1152\n"
    "ds_read_b64 v[50:51], v0 offset:1160\n"
    "ds_read_b64 v[52:53], v0 offset:1664\n"
    "ds_read_b64 v[54:55], v0 offset:1672\n"
    "ds_read_b64 v[56:57], v0 offset:192\n"
    "ds_read_b64 v[58:59], v0 offset:200\n"
    "ds_read_b64 v[60:61], v0 offset:704\n"
    "ds_read_b64 v[62:63], v0 offset:712\n"
    "ds_read_b64 v[64:65], v0 offset:1216\n"
    "ds_read_b64 v[66:67], v0 offset:1224\n"
    "ds_read_b64 v[68:69], v0 offset:1728\n"
    "ds_read_b64 v[70:71], v0 offset:1736\n"
    "ds_read_b64 v[72:73], v0 offset:256\n"
    "ds_read_b64 v[74:75], v0 offset:264\n"
    "ds_read_b64 v[76:77], v0 offset:768\n"
    "ds_read_b64 v[78:79], v0 offset:776\n"
    "ds_read_b64 v[80:81], v0 offset:1280\n"
    "ds_read_b64 v[82:83], v0 offset:1288\n"
    "ds_read_b64 v[84:85], v0 offset:1792\n"
    "ds_read_b64 v[86:87], v0 offset:1800\n"
    "ds_read_b64 v[88:89], v0 offset:320\n"
    "ds_read_b64 v[90:91], v0 offset:328\n"
    "ds_read_b64 v[92:93], v0 offset:832\n"
    "ds_read_b64 v[94:95], v0 offset:840\n"
    "ds_read_b64 v[96:97], v0 offset:1344\n"
    "ds_read_b64 v[98:99], v0 offset:1352\n"
    "ds_read_b64 v[100:101], v0 offset:1856\n"
    "ds_read_b64 v[102:103], v0 offset:1864\n"
    "ds_read_b64 v[104:105], v0 offset:384\n"
    "ds_read_b64 v[106:107], v0 offset:392\n"
    "ds_read_b64 v[108:109], v0 offset:896\n"
    "ds_read_b64 v[110:111], v0 offset:904\n"
    "ds_read_b64 v[112:113], v0 offset:1408\n"
    "ds_read_b64 v[114:115], v0 offset:1416\n"
    "ds_read_b64 v[116:117], v0 offset:1920\n"
    "ds_read_b64 v[118:119], v0 offset:1928\n"
    "ds_read_b64 v[120:121], v0 offset:448\n"
    "ds_read_b64 v[122:123], v0 offset:456\n"
    "ds_read_b64 v[124:125], v0 offset:960\n"
    "ds_read_b64 v[126:127], v0 offset:968\n"
    "ds_read_b64 v[128:129], v0 offset:1472\n"
    "ds_read_b64 v[130:131], v0 offset:1480\n"
    "ds_read_b64 v[132:133], v0 offset:1984\n"
    "ds_read_b64 v[134:135], v0 offset:1992\n"
    "s_waitcnt lgkmcnt(0)\n"
    "Lt38:\n"
    "s_sleep 2\n"
    "s_memrealtime s[6:7]\n"
    "s_waitcnt lgkmcnt(0)\n"
    "s_sub_u32 s5, s6, s100\n"
    "s_cmp_lt_u32 s5, 325\n"
    "s_cbranch_scc1 Lt38\n"
    "s_mov_b32 s4, 4194304\n"
    "Lh1c:\n"
    "s_waitcnt lgkmcnt(0)\n"
    "ds_read_b32 v4, v3 offset:0\n"
    "ds_read_b128 v[136:139], v1 offset:448\n"
    "ds_read_b128 v[140:143], v1 offset:960\n"
    "ds_read_b128 v[144:147], v1 offset:1472\n"
    "s_waitcnt lgkmcnt(3)\n"
    "v_readfirstlane_b32 s5, v4\n"
    "s_sub_u32 s4, s4, 1\n"
    "s_cbranch_scc1 Lh1cx\n"
    "s_cmp_lg_u32 s5, 0x01010101\n"
    "s_cbranch_scc1 Lh1c\n"
    "Lh1cx:\n"
    "s_waitcnt lgkmcnt(2)\n"
    "v_mfma_f32_16x16x32_bf16 v[148:151], v[8:11], v[136:139], 0\n"
    "v_mfma_f32_16x16x32_bf16 v[152:155], v[12:15], v[136:139], 0\n"
    "ds_read_b128 v[156:159], v1 offset:1984\n"
    "s_waitcnt lgkmcnt(2)\n"
    "v_mfma_f32_16x16x32_bf16 v[152:155], v[8:11], v[140:143], v[152:155]\n"
    "ds_read_b128 v[160:163], v1 offset:384\n"
    "v_mfma_f32_16x16x32_bf16 v[164:167], v[16:19], v[136:139], 0\n"
    "v_mfma_f32_16x16x32_bf16 v[164:167], v[12:15], v[140:143], v[164:167]\n"
    "ds_read_b128 v[168:171], v1 offset:896\n"
    "s_waitcnt lgkmcnt(3)\n"
    "v_mfma_f32_16x16x32_bf16 v[164:167], v[8:11], v[144:147], v[164:167]\n"
    "v_mfma_f32_16x16x32_bf16 v[172:175], v[20:23], v[136:139], 0\n"
    "v_mfma_f32_16x16x32_bf16 v[172:175], v[16:19], v[140:143], v[172:175]\n"
    "ds_read_b128 v[136:139], v1 offset:1408\n"
    "v_mfma_f32_16x16x32_bf16 v[172:175], v[12:15], v[144:147], v[172:175]\n"
    "s_waitcnt lgkmcnt(2)\n"
    "v_mfma_f32_16x16x32_bf16 v[172:175], v[8:11], v[156:159], v[172:175]\n"
    "v_mfma_f32_16x16x32_bf16 v[148:151], v[24:27], v[160:163], v[148:151]\n"
    "v_mfma_f32_16x16x32_bf16 v[152:155], v[28:31], v[160:163], v[152:155]\n"
    "ds_read_b128 v[140:143], v1 offset:1920\n"
    "s_waitcnt lgkmcnt(2)\n"
    "v_mfma_f32_16x16x32_bf16 v[152:155], v[24:27], v[168:171], v[152:155]\n"
    "ds_read_b128 v[144:147], v1 offset:320\n"
    "v_mfma_f32_16x16x32_bf16 v[164:167], v[32:35], v[160:163], v[164:167]\n"
    "v_mfma_f32_16x16x32_bf16 v[164:167], v[28:31], v[168:171], v[164:167]\n"
    "ds_read_b128 v[156:159], v1 offset:832\n"
    "s_waitcnt lgkmcnt(3)\n"
    "v_mfma_f32_16x16x32_bf16 v[164:167], v[24:27], v[136:139], v[164:167]\n"
    "v_mfma_f32_16x16x32_bf16 v[172:175], v[36:39], v[160:163], v[172:175]\n"
    "v_mfma_f32_16x16x32_bf16 v[172:175], v[32:35], v[168:171], v[172:175]\n"
    "ds_read_b128 v[160:163], v1 offset:1344\n"
    "v_mfma_f32_16x16x32_bf16 v[172:175], v[28:31], v[136:139], v[172:175]\n"
    "s_waitcnt lgkmcnt(2)\n"
    "v_mfma_f32_16x16x32_bf16 v[172:175], v[24:27], v[140:143], v[172:175]\n"
    "v_mfma_f32_16x16x32_bf16 v[148:151], v[40:43], v[144:147], v[148:151]\n"
    "v_mfma_f32_16x16x32_bf16 v[152:155], v[44:47], v[144:147], v[152:155]\n"
    "ds_read_b128 v[168:171], v1 offset:1856\n"
    "s_waitcnt lgkmcnt(2)\n"
    "v_mfma_f32_16x16x32_bf16 v[152:155], v[40:43], v[156:159], v[152:155]\n"
    "ds_read_b128 v[136:139], v1 offset:256\n"
    "v_mfma_f32_16x16x32_bf16 v[164:167], v[48:51], v[144:147], v[164:167]\n"
    "v_mfma_f32_16x16x32_bf16 v[164:167], v[44:47], v[156:159], v[164:167]\n"
    "ds_read_b128 v[140:143], v1 offset:768\n"
    "s_waitcnt lgkmcnt(3)\n"
    "v_mfma_f32_16x16x32_bf16 v[164:167], v[40:43], v[160:163], v[164:167]\n"
    "v_mfma_f32_16x16x32_bf16 v[172:175], v[52:55], v[144:147], v[172:175]\n"
    "v_mfma_f32_16x16x32_bf16 v[172:175], v[48:51], v[156:159], v[172:175]\n"
    "ds_read_b128 v[144:147], v1 offset:1280\n"
    "v_mfma_f32_16x16x32_bf16 v[172:175], v[44:47], v[160:163], v[172:175]\n"
    "s_waitcnt lgkmcnt(2)\n"
    "v_mfma_f32_16x16x32_bf16 v[172:175], v[40:43], v[168:171], v[172:175]\n"
    "v_mfma_f32_16x16x32_bf16 v[148:151], v[56:59], v[136:139], v[148:151]\n"
    "v_mfma_f32_16x16x32_bf16 v[152:155], v[60:63], v[136:139], v[152:155]\n"
    "ds_read_b128 v[156:159], v1 offset:1792\n"
    "s_waitcnt lgkmcnt(2)\n"
    "v_mfma_f32_16x16x32_bf16 v[152:155], v[56:59], v[140:143], v[152:155]\n"
    "ds_read_b128 v[160:163], v1 offset:192\n"
    "v_mfma_f32_16x16x32_bf16 v[164:167], v[64:67], v[136:139], v[164:167]\n"
    "v_mfma_f32_16x16x32_bf16 v[164:167], v[60:63], v[140:143], v[164:167]\n"
    "ds_read_b128 v[168:171], v1 offset:704\n"
    "s_waitcnt lgkmcnt(3)\n"
    "v_mfma_f32_16x16x32_bf16 v[164:167], v[56:59], v[144:147], v[164:167]\n"
    "v_mfma_f32_16x16x32_bf16 v[172:175], v[68:71], v[136:139], v[172:175]\n"
    "v_mfma_f32_16x16x32_bf16 v[172:175], v[64:67], v[140:143], v[172:175]\n"
    "ds_read_b128 v[136:139], v1 offset:1216\n"
    "v_mfma_f32_16x16x32_bf16 v[172:175], v[60:63], v[144:147], v[172:175]\n"
    "s_waitcnt lgkmcnt(2)\n"
    "v_mfma_f32_16x16x32_bf16 v[172:175], v[56:59], v[156:159], v[172:175]\n"
    "v_mfma_f32_16x16x32_bf16 v[148:151], v[72:75], v[160:163], v[148:151]\n"
    "v_mfma_f32_16x16x32_bf16 v[152:155], v[76:79], v[160:163], v[152:155]\n"
    "ds_read_b128 v[140:143], v1 offset:1728\n"
    "s_waitcnt lgkmcnt(2)\n"
    "v_mfma_f32_16x16x32_bf16 v[152:155], v[72:75], v[168:171], v[152:155]\n"
    "ds_read_b128 v[144:147], v1 offset:128\n"
    "v_mfma_f32_16x16x32_bf16 v[164:167], v[80:83], v[160:163], v[164:167]\n"
    "v_mfma_f32_16x16x32_bf16 v[164:167], v[76:79], v[168:171], v[164:167]\n"
    "ds_read_b128 v[156:159], v1 offset:640\n"
    "s_waitcnt lgkmcnt(3)\n"
    "v_mfma_f32_16x16x32_bf16 v[164:167], v[72:75], v[136:139], v[164:167]\n"
    "v_mfma_f32_16x16x32_bf16 v[172:175], v[84:87], v[160:163], v[172:175]\n"
    "v_mfma_f32_16x16x32_bf16 v[172:175], v[80:83], v[168:171], v[172:175]\n"
    "ds_read_b128 v[160:163], v1 offset:1152\n"
    "v_mfma_f32_16x16x32_bf16 v[172:175], v[76:79], v[136:139], v[172:175]\n"
    "s_waitcnt lgkmcnt(2)\n"
    "v_mfma_f32_16x16x32_bf16 v[172:175], v[72:75], v[140:143], v[172:175]\n"
    "v_mfma_f32_16x16x32_bf16 v[148:151], v[88:91], v[144:147], v[148:151]\n"
    "v_mfma_f32_16x16x32_bf16 v[152:155], v[92:95], v[144:147], v[152:155]\n"
    "ds_read_b128 v[168:171], v1 offset:1664\n"
    "s_waitcnt lgkmcnt(2)\n"
    "v_mfma_f32_16x16x32_bf16 v[152:155], v[88:91], v[156:159], v[152:155]\n"
    "ds_read_b128 v[136:139], v1 offset:64\n"
    "v_mfma_f32_16x16x32_bf16 v[164:167], v[96:99], v[144:147], v[164:167]\n"
    "v_mfma_f32_16x16x32_bf16 v[164:167], v[92:95], v[156:159], v[164:167]\n"
    "ds_read_b128 v[140:143], v1 offset:576\n"
    "s_waitcnt lgkmcnt(3)\n"
    "v_mfma_f32_16x16x32_bf16 v[164:167], v[88:91], v[160:163], v[164:167]\n"
    "v_mfma_f32_16x16x32_bf16 v[172:175], v[100:103], v[144:147], v[172:175]\n"
    "v_mfma_f32_16x16x32_bf16 v[172:175], v[96:99], v[156:159], v[172:175]\n"
    "ds_read_b128 v[144:147], v1 offset:1088\n"
    "v_mfma_f32_16x16x32_bf16 v[172:175], v[92:95], v[160:163], v[172:175]\n"
    "s_waitcnt lgkmcnt(2)\n"
    "v_mfma_f32_16x16x32_bf16 v[172:175], v[88:91], v[168:171], v[172:175]\n"
    "v_mfma_f32_16x16x32_bf16 v[148:151], v[104:107], v[136:139], v[148:151]\n"
    "v_mfma_f32_16x16x32_bf16 v[152:155], v[108:111], v[136:139], v[152:155]\n"
    "ds_read_b128 v[156:159], v1 offset:1600\n"
    "s_waitcnt lgkmcnt(2)\n"
    "v_mfma_f32_16x16x32_bf16 v[152:155], v[104:107], v[140:143], v[152:155]\n"
    "ds_read_b128 v[160:163], v1 offset:0\n"
    "v_mfma_f32_16x16x32_bf16 v[164:167], v[112:115], v[136:139], v[164:167]\n"
    "v_mfma_f32_16x16x32_bf16 v[164:167], v[108:111], v[140:143], v[164:167]\n"
    "ds_read_b128 v[168:171], v1 offset:512\n"
    "s_waitcnt lgkmcnt(3)\n"
    "v_mfma_f32_16x16x32_bf16 v[164:167], v[104:107], v[144:147], v[164:167]\n"
    "v_mfma_f32_16x16x32_bf16 v[172:175], v[116:119], v[136:139], v[172:175]\n"
    "v_mfma_f32_16x16x32_bf16 v[172:175], v[112:115], v[140:143], v[172:175]\n"
    "ds_read_b128 v[136:139], v1 offset:1024\n"
    "v_mfma_f32_16x16x32_bf16 v[172:175], v[108:111], v[144:147], v[172:175]\n"
    "ds_read_b128 v[140:143], v1 offset:1536\n"
    "s_waitcnt lgkmcnt(3)\n"
    "v_mfma_f32_16x16x32_bf16 v[172:175], v[104:107], v[156:159], v[172:175]\n"
    "ds_read_b64 v[144:145], v0 offset:2048\n"
    "v_mfma_f32_16x16x32_bf16 v[148:151], v[120:123], v[160:163], v[148:151]\n"
    "ds_read_b64 v[146:147], v0 offset:2056\n"
    "v_mfma_f32_16x16x32_bf16 v[152:155], v[124:127], v[160:163], v[152:155]\n"
    "ds_read_b128 v[156:159], v1 offset:448\n"
    "s_waitcnt lgkmcnt(5)\n"
    "v_mfma_f32_16x16x32_bf16 v[152:155], v[120:123], v[168:171], v[152:155]\n"
    "ds_read_b128 v[176:179], v1 offset:960\n"
    "v_mfma_f32_16x16x32_bf16 v[164:167], v[128:131], v[160:163], v[164:167]\n"
    "ds_read_b128 v[180:183], v1 offset:1472\n"
    "v_mfma_f32_16x16x32_bf16 v[164:167], v[124:127], v[168:171], v[164:167]\n"
    "ds_read_b128 v[184:187], v1 offset:1984\n"
    "s_waitcnt lgkmcnt(7)\n"
    "v_mfma_f32_16x16x32_bf16 v[164:167], v[120:123], v[136:139], v[164:167]\n"
    "ds_read_b64 v[188:189], v0 offset:2560\n"
    "v_mfma_f32_16x16x32_bf16 v[172:175], v[132:135], v[160:163], v[172:175]\n"
    "ds_read_b64 v[190:191], v0 offset:2568\n"
    "v_mfma_f32_16x16x32_bf16 v[172:175], v[128:131], v[168:171], v[172:175]\n"
    "v_mfma_f32_16x16x32_bf16 v[172:175], v[124:127], v[136:139], v[172:175]\n"
    "s_waitcnt lgkmcnt(5)\n"
    "v_mfma_f32_16x16x32_bf16 v[172:175], v[120:123], v[140:143], v[172:175]\n"
    "ds_read_b64 v[160:161], v0 offset:3072\n"
    "v_mfma_f32_16x16x32_bf16 v[168:171], v[144:147], v[156:159], 1.0\n"
    "ds_read_b64 v[162:163], v0 offset:3080\n"
    "s_waitcnt lgkmcnt(5)\n"
    "v_mfma_f32_16x16x32_bf16 v[168:171], v[20:23], v[176:179], v[168:171]\n"
    "ds_read_b64 v[136:137], v0 offset:3584\n"
    "v_mfma_f32_16x16x32_bf16 v[168:171], v[16:19], v[180:183], v[168:171]\n"
    "ds_read_b64 v[138:139], v0 offset:3592\n"
    "s_waitcnt lgkmcnt(4)\n"
    "v_mfma_f32_16x16x32_bf16 v[168:171], v[12:15], v[184:187], v[168:171]\n"
    "ds_read_b64 v[140:141], v0 offset:2112\n"
    "v_mfma_f32_16x16x32_bf16 v[192:195], v[188:191], v[156:159], 1.0\n"
    "ds_read_b64 v[142:143], v0 offset:2120\n"
    "ds_write_b128 v2, v[148:151] offset:0\n"
    "v_mfma_f32_16x16x32_bf16 v[192:195], v[144:147], v[176:179], v[192:195]\n"
    "ds_read_b128 v[196:199], v1 offset:384\n"
    "ds_write_b128 v2, v[152:155] offset:1280\n"
    "v_mfma_f32_16x16x32_bf16 v[192:195], v[20:23], v[180:183], v[192:195]\n"
    "ds_read_b128 v[200:203], v1 offset:896\n"
    "ds_write_b128 v2, v[164:167] offset:2560\n"
    "v_mfma_f32_16x16x32_bf16 v[192:195], v[16:19], v[184:187], v[192:195]\n"
    "ds_read_b128 v[204:207], v1 offset:1408\n"
    "ds_write_b128 v2, v[172:175] offset:3840\n"
    "ds_write_b8 v6, v5 offset:512\n"
    "s_waitcnt lgkmcnt(12)\n"
    "v_mfma_f32_16x16x32_bf16 v[184:187], v[160:163], v[156:159], 1.0\n"
    "ds_read_b128 v[208:211], v1 offset:1920\n"
    "v_mfma_f32_16x16x32_bf16 v[184:187], v[188:191], v[176:179], v[184:187]\n"
    "ds_read_b64 v[212:213], v0 offset:2624\n"
    "v_mfma_f32_16x16x32_bf16 v[184:187], v[144:147], v[180:183], v[184:187]\n"
    "ds_read_b64 v[214:215], v0 offset:2632\n"
    "s_waitcnt lgkmcnt(13)\n"
    "v_mfma_f32_16x16x32_bf16 v[148:151], v[136:139], v[156:159], 1.0\n"
    "v_mfma_f32_16x16x32_bf16 v[148:151], v[160:163], v[176:179], v[148:151]\n"
    "v_mfma_f32_16x16x32_bf16 v[148:151], v[188:191], v[180:183], v[148:151]\n"
    "ds_read_b64 v[152:153], v0 offset:3136\n"
    "s_waitcnt lgkmcnt(8)\n"
    "v_mfma_f32_16x16x32_bf16 v[168:171], v[140:143], v[196:199], v[168:171]\n"
    "ds_read_b64 v[154:155], v0 offset:3144\n"
    "v_mfma_f32_16x16x32_bf16 v[168:171], v[36:39], v[200:203], v[168:171]\n"
    "ds_read_b64 v[164:165], v0 offset:3648\n"
    "s_waitcnt lgkmcnt(5)\n"
    "v_mfma_f32_16x16x32_bf16 v[168:171], v[32:35], v[204:207], v[168:171]\n"
    "ds_read_b64 v[166:167], v0 offset:3656\n"
    "v_mfma_f32_16x16x32_bf16 v[168:171], v[28:31], v[208:211], v[168:171]\n"
    "ds_read_b64 v[172:173], v0 offset:2176\n"
    "s_waitcnt lgkmcnt(5)\n"
    "v_mfma_f32_16x16x32_bf16 v[192:195], v[212:215], v[196:199], v[192:195]\n"
    "ds_read_b64 v[174:175], v0 offset:2184\n"
    "v_mfma_f32_16x16x32_bf16 v[192:195], v[140:143], v[200:203], v[192:195]\n"
    "ds_read_b128 v[136:139], v1 offset:320\n"
    "v_mfma_f32_16x16x32_bf16 v[192:195], v[36:39], v[204:207], v[192:195]\n"
    "ds_read_b128 v[156:159], v1 offset:832\n"
    "v_mfma_f32_16x16x32_bf16 v[192:195], v[32:35], v[208:211], v[192:195]\n"
    "ds_read_b128 v[160:163], v1 offset:1344\n"
    "s_waitcnt lgkmcnt(7)\n"
    "v_mfma_f32_16x16x32_bf16 v[184:187], v[152:155], v[196:199], v[184:187]\n"
    "ds_read_b128 v[176:179], v1 offset:1856\n"
    "v_mfma_f32_16x16x32_bf16 v[184:187], v[212:215], v[200:203], v[184:187]\n"
    "ds_read_b64 v[188:189], v0 offset:2688\n"
    "v_mfma_f32_16x16x32_bf16 v[184:187], v[140:143], v[204:207], v[184:187]\n"
    "ds_read_b64 v[190:191], v0 offset:2696\n"
    "s_waitcnt lgkmcnt(8)\n"
    "v_mfma_f32_16x16x32_bf16 v[148:151], v[164:167], v[196:199], v[148:151]\n"
    "v_mfma_f32_16x16x32_bf16 v[148:151], v[152:155], v[200:203], v[148:151]\n"
    "v_mfma_f32_16x16x32_bf16 v[148:151], v[212:215], v[204:207], v[148:151]\n"
    "ds_read_b64 v[180:181], v0 offset:3200\n"
    "s_waitcnt lgkmcnt(5)\n"
    "v_mfma_f32_16x16x32_bf16 v[168:171], v[172:175], v[136:139], v[168:171]\n"
    "ds_read_b64 v[182:183], v0 offset:3208\n"
    "v_mfma_f32_16x16x32_bf16 v[168:171], v[52:55], v[156:159], v[168:171]\n"
    "ds_read_b64 v[208:209], v0 offset:3712\n"
    "s_waitcnt lgkmcnt(5)\n"
    "v_mfma_f32_16x16x32_bf16 v[168:171], v[48:51], v[160:163], v[168:171]\n"
    "ds_read_b64 v[210:211], v0 offset:3720\n"
    "v_mfma_f32_16x16x32_bf16 v[168:171], v[44:47], v[176:179], v[168:171]\n"
    "ds_read_b64 v[164:165], v0 offset:2240\n"
    "s_waitcnt lgkmcnt(5)\n"
    "v_mfma_f32_16x16x32_bf16 v[192:195], v[188:191], v[136:139], v[192:195]\n"
    "ds_read_b64 v[166:167], v0 offset:2248\n"
    "v_mfma_f32_16x16x32_bf16 v[192:195], v[172:175], v[156:159], v[192:195]\n"
    "ds_read_b128 v[196:199], v1 offset:256\n"
    "v_mfma_f32_16x16x32_bf16 v[192:195], v[52:55], v[160:163], v[192:195]\n"
    "ds_read_b128 v[152:155], v1 offset:768\n"
    "v_mfma_f32_16x16x32_bf16 v[192:195], v[48:51], v[176:179], v[192:195]\n"
    "ds_read_b128 v[200:203], v1 offset:1280\n"
    "s_waitcnt lgkmcnt(7)\n"
    "v_mfma_f32_16x16x32_bf16 v[184:187], v[180:183], v[136:139], v[184:187]\n"
    "ds_read_b128 v[212:215], v1 offset:1792\n"
    "v_mfma_f32_16x16x32_bf16 v[184:187], v[188:191], v[156:159], v[184:187]\n"
    "ds_read_b64 v[204:205], v0 offset:2752\n"
    "v_mfma_f32_16x16x32_bf16 v[184:187], v[172:175], v[160:163], v[184:187]\n"
    "ds_read_b64 v[206:207], v0 offset:2760\n"
    "s_waitcnt lgkmcnt(8)\n"
    "v_mfma_f32_16x16x32_bf16 v[148:151], v[208:211], v[136:139], v[148:151]\n"
    "v_mfma_f32_16x16x32_bf16 v[148:151], v[180:183], v[156:159], v[148:151]\n"
    "v_mfma_f32_16x16x32_bf16 v[148:151], v[188:191], v[160:163], v[148:151]\n"
    "ds_read_b64 v[176:177], v0 offset:3264\n"
    "s_waitcnt lgkmcnt(5)\n"
    "v_mfma_f32_16x16x32_bf16 v[168:171], v[164:167], v[196:199], v[168:171]\n"
    "ds_read_b64 v[178:179], v0 offset:3272\n"
    "v_mfma_f32_16x16x32_bf16 v[168:171], v[68:71], v[152:155], v[168:171]\n"
    "ds_read_b64 v[208:209], v0 offset:3776\n"
    "s_waitcnt lgkmcnt(5)\n"
    "v_mfma_f32_16x16x32_bf16 v[168:171], v[64:67], v[200:203], v[168:171]\n"
    "ds_read_b64 v[210:211], v0 offset:3784\n"
    "v_mfma_f32_16x16x32_bf16 v[168:171], v[60:63], v[212:215], v[168:171]\n"
    "ds_read_b64 v[136:137], v0 offset:2304\n"
    "s_waitcnt lgkmcnt(5)\n"
    "v_mfma_f32_16x16x32_bf16 v[192:195], v[204:207], v[196:199], v[192:195]\n"
    "ds_read_b64 v[138:139], v0 offset:2312\n"
    "v_mfma_f32_16x16x32_bf16 v[192:195], v[164:167], v[152:155], v[192:195]\n"
    "ds_read_b128 v[180:183], v1 offset:192\n"
    "v_mfma_f32_16x16x32_bf16 v[192:195], v[68:71], v[200:203], v[192:195]\n"
    "ds_read_b128 v[156:159], v1 offset:704\n"
    "v_mfma_f32_16x16x32_bf16 v[192:195], v[64:67], v[212:215], v[192:195]\n"
    "ds_read_b128 v[188:191], v1 offset:1216\n"
    "s_waitcnt lgkmcnt(7)\n"
    "v_mfma_f32_16x16x32_bf16 v[184:187], v[176:179], v[196:199], v[184:187]\n"
    "ds_read_b128 v[160:163], v1 offset:1728\n"
    "v_mfma_f32_16x16x32_bf16 v[184:187], v[204:207], v[152:155], v[184:187]\n"
    "ds_read_b64 v[212:213], v0 offset:2816\n"
    "v_mfma_f32_16x16x32_bf16 v[184:187], v[164:167], v[200:203], v[184:187]\n"
    "ds_read_b64 v[214:215], v0 offset:2824\n"
    "s_waitcnt lgkmcnt(8)\n"
    "v_mfma_f32_16x16x32_bf16 v[148:151], v[208:211], v[196:199], v[148:151]\n"
    "v_mfma_f32_16x16x32_bf16 v[148:151], v[176:179], v[152:155], v[148:151]\n"
    "v_mfma_f32_16x16x32_bf16 v[148:151], v[204:207], v[200:203], v[148:151]\n"
    "ds_read_b64 v[208:209], v0 offset:3328\n"
    "s_waitcnt lgkmcnt(5)\n"
    "v_mfma_f32_16x16x32_bf16 v[168:171], v[136:139], v[180:183], v[168:171]\n"
    "ds_read_b64 v[210:211], v0 offset:3336\n"
    "v_mfma_f32_16x16x32_bf16 v[168:171], v[84:87], v[156:159], v[168:171]\n"
    "ds_read_b64 v[196:197], v0 offset:3840\n"
    "s_waitcnt lgkmcnt(5)\n"
    "v_mfma_f32_16x16x32_bf16 v[168:171], v[80:83], v[188:191], v[168:171]\n"
    "ds_read_b64 v[198:199], v0 offset:3848\n"
    "v_mfma_f32_16x16x32_bf16 v[168:171], v[76:79], v[160:163], v[168:171]\n"
    "ds_read_b64 v[176:177], v0 offset:2368\n"
    "s_waitcnt lgkmcnt(5)\n"
    "v_mfma_f32_16x16x32_bf16 v[192:195], v[212:215], v[180:183], v[192:195]\n"
    "ds_read_b64 v[178:179], v0 offset:2376\n"
    "v_mfma_f32_16x16x32_bf16 v[192:195], v[136:139], v[156:159], v[192:195]\n"
    "ds_read_b128 v[152:155], v1 offset:128\n"
    "v_mfma_f32_16x16x32_bf16 v[192:195], v[84:87], v[188:191], v[192:195]\n"
    "ds_read_b128 v[204:207], v1 offset:640\n"
    "v_mfma_f32_16x16x32_bf16 v[192:195], v[80:83], v[160:163], v[192:195]\n"
    "ds_read_b128 v[200:203], v1 offset:1152\n"
    "s_waitcnt lgkmcnt(7)\n"
    "v_mfma_f32_16x16x32_bf16 v[184:187], v[208:211], v[180:183], v[184:187]\n"
    "ds_read_b128 v[160:163], v1 offset:1664\n"
    "v_mfma_f32_16x16x32_bf16 v[184:187], v[212:215], v[156:159], v[184:187]\n"
    "ds_read_b64 v[216:217], v0 offset:2880\n"
    "v_mfma_f32_16x16x32_bf16 v[184:187], v[136:139], v[188:191], v[184:187]\n"
    "ds_read_b64 v[218:219], v0 offset:2888\n"
    "s_waitcnt lgkmcnt(8)\n"
    "v_mfma_f32_16x16x32_bf16 v[148:151], v[196:199], v[180:183], v[148:151]\n"
    "v_mfma_f32_16x16x32_bf16 v[148:151], v[208:211], v[156:159], v[148:151]\n"
    "v_mfma_f32_16x16x32_bf16 v[148:151], v[212:215], v[188:191], v[148:151]\n"
    "ds_read_b64 v[196:197], v0 offset:3392\n"
    "s_waitcnt lgkmcnt(5)\n"
    "v_mfma_f32_16x16x32_bf16 v[168:171], v[176:179], v[152:155], v[168:171]\n"
    "ds_read_b64 v[198:199], v0 offset:3400\n"
    "v_mfma_f32_16x16x32_bf16 v[168:171], v[100:103], v[204:207], v[168:171]\n"
    "ds_read_b64 v[180:181], v0 offset:3904\n"
    "s_waitcnt lgkmcnt(5)\n"
    "v_mfma_f32_16x16x32_bf16 v[168:171], v[96:99], v[200:203], v[168:171]\n"
    "ds_read_b64 v[182:183], v0 offset:3912\n"
    "v_mfma_f32_16x16x32_bf16 v[168:171], v[92:95], v[160:163], v[168:171]\n"
    "ds_read_b64 v[208:209], v0 offset:2432\n"
    "s_waitcnt lgkmcnt(5)\n"
    "v_mfma_f32_16x16x32_bf16 v[192:195], v[216:219], v[152:155], v[192:195]\n"
    "ds_read_b64 v[210:211], v0 offset:2440\n"
    "v_mfma_f32_16x16x32_bf16 v[192:195], v[176:179], v[204:207], v[192:195]\n"
    "ds_read_b128 v[156:159], v1 offset:64\n"
    "v_mfma_f32_16x16x32_bf16 v[192:195], v[100:103], v[200:203], v[192:195]\n"
    "ds_read_b128 v[212:215], v1 offset:576\n"
    "v_mfma_f32_16x16x32_bf16 v[192:195], v[96:99], v[160:163], v[192:195]\n"
    "ds_read_b128 v[188:191], v1 offset:1088\n"
    "s_waitcnt lgkmcnt(7)\n"
    "v_mfma_f32_16x16x32_bf16 v[184:187], v[196:199], v[152:155], v[184:187]\n"
    "ds_read_b128 v[160:163], v1 offset:1600\n"
    "v_mfma_f32_16x16x32_bf16 v[184:187], v[216:219], v[204:207], v[184:187]\n"
    "ds_read_b64 v[220:221], v0 offset:2944\n"
    "v_mfma_f32_16x16x32_bf16 v[184:187], v[176:179], v[200:203], v[184:187]\n"
    "ds_read_b64 v[222:223], v0 offset:2952\n"
    "s_waitcnt lgkmcnt(8)\n"
    "v_mfma_f32_16x16x32_bf16 v[148:151], v[180:183], v[152:155], v[148:151]\n"
    "v_mfma_f32_16x16x32_bf16 v[148:151], v[196:199], v[204:207], v[148:151]\n"
    "v_mfma_f32_16x16x32_bf16 v[148:151], v[216:219], v[200:203], v[148:151]\n"
    "ds_read_b64 v[180:181], v0 offset:3456\n"
    "s_waitcnt lgkmcnt(5)\n"
    "v_mfma_f32_16x16x32_bf16 v[168:171], v[208:211], v[156:159], v[168:171]\n"
    "ds_read_b64 v[182:183], v0 offset:3464\n"
    "v_mfma_f32_16x16x32_bf16 v[168:171], v[116:119], v[212:215], v[168:171]\n"
    "ds_read_b64 v[152:153], v0 offset:3968\n"
    "s_waitcnt lgkmcnt(5)\n"
    "v_mfma_f32_16x16x32_bf16 v[168:171], v[112:115], v[188:191], v[168:171]\n"
    "ds_read_b64 v[154:155], v0 offset:3976\n"
    "v_mfma_f32_16x16x32_bf16 v[168:171], v[108:111], v[160:163], v[168:171]\n"
    "ds_read_b64 v[196:197], v0 offset:2496\n"
    "s_waitcnt lgkmcnt(5)\n"
    "v_mfma_f32_16x16x32_bf16 v[192:195], v[220:223], v[156:159], v[192:195]\n"
    "ds_read_b64 v[198:199], v0 offset:2504\n"
    "v_mfma_f32_16x16x32_bf16 v[192:195], v[208:211], v[212:215], v[192:195]\n"
    "ds_read_b128 v[204:207], v1 offset:0\n"
    "v_mfma_f32_16x16x32_bf16 v[192:195], v[116:119], v[188:191], v[192:195]\n"
    "ds_read_b128 v[216:219], v1 offset:512\n"
    "v_mfma_f32_16x16x32_bf16 v[192:195], v[112:115], v[160:163], v[192:195]\n"
    "ds_read_b128 v[200:203], v1 offset:1024\n"
    "s_waitcnt lgkmcnt(7)\n"
    "v_mfma_f32_16x16x32_bf16 v[184:187], v[180:183], v[156:159], v[184:187]\n"
    "ds_read_b128 v[160:163], v1 offset:1536\n"
    "v_mfma_f32_16x16x32_bf16 v[184:187], v[220:223], v[212:215], v[184:187]\n"
    "ds_read_b64 v[224:225], v0 offset:3008\n"
    "v_mfma_f32_16x16x32_bf16 v[184:187], v[208:211], v[188:191], v[184:187]\n"
    "ds_read_b64 v[226:227], v0 offset:3016\n"
    "s_waitcnt lgkmcnt(8)\n"
    "v_mfma_f32_16x16x32_bf16 v[148:151], v[152:155], v[156:159], v[148:151]\n"
    "ds_read_b32 v4, v3 offset:256\n"
    "v_mfma_f32_16x16x32_bf16 v[148:151], v[180:183], v[212:215], v[148:151]\n"
    "v_mfma_f32_16x16x32_bf16 v[148:151], v[220:223], v[188:191], v[148:151]\n"
    "ds_read_b64 v[152:153], v0 offset:3520\n"
    "s_waitcnt lgkmcnt(6)\n"
    "v_mfma_f32_16x16x32_bf16 v[168:171], v[196:199], v[204:207], v[168:171]\n"
    "ds_read_b64 v[154:155], v0 offset:3528\n"
    "v_mfma_f32_16x16x32_bf16 v[168:171], v[132:135], v[216:219], v[168:171]\n"
    "s_waitcnt lgkmcnt(5)\n"
    "v_mfma_f32_16x16x32_bf16 v[168:171], v[128:131], v[200:203], v[168:171]\n"
    "ds_read_b64 v[156:157], v0 offset:4032\n"
    "v_mfma_f32_16x16x32_bf16 v[168:171], v[124:127], v[160:163], v[168:171]\n"
    "ds_read_b64 v[158:159], v0 offset:4040\n"
    "s_waitcnt lgkmcnt(5)\n"
    "v_mfma_f32_16x16x32_bf16 v[192:195], v[224:227], v[204:207], v[192:195]\n"
    "v_mfma_f32_16x16x32_bf16 v[192:195], v[196:199], v[216:219], v[192:195]\n"
    "s_waitcnt lgkmcnt(4)\n"
    "v_readfirstlane_b32 s5, v4\n"
    "s_cmp_lg_u32 s5, 0x01010101\n"
    "s_cbranch_scc1 Lh2c\n"
    "Lh2rc:\n"
    "v_mfma_f32_16x16x32_bf16 v[192:195], v[132:135], v[200:203], v[192:195]\n"
    "ds_read_b128 v[180:183], v1 offset:2496\n"
    "v_mfma_f32_16x16x32_bf16 v[192:195], v[128:131], v[160:163], v[192:195]\n"
    "s_waitcnt lgkmcnt(3)\n"
    "v_mfma_f32_16x16x32_bf16 v[184:187], v[152:155], v[204:207], v[184:187]\n"
    "ds_read_b128 v[212:215], v1 offset:3008\n"
    "v_mfma_f32_16x16x32_bf16 v[184:187], v[224:227], v[216:219], v[184:187]\n"
    "ds_read_b128 v[220:223], v1 offset:2432\n"
    "v_mfma_f32_16x16x32_bf16 v[184:187], v[196:199], v[200:203], v[184:187]\n"
    "s_waitcnt lgkmcnt(3)\n"
    "v_mfma_f32_16x16x32_bf16 v[148:151], v[156:159], v[204:207], v[148:151]\n"
    "ds_read_b128 v[188:191], v1 offset:2944\n"
    "v_mfma_f32_16x16x32_bf16 v[148:151], v[152:155], v[216:219], v[148:151]\n"
    "ds_read_b128 v[160:163], v1 offset:2368\n"
    "v_mfma_f32_16x16x32_bf16 v[148:151], v[224:227], v[200:203], v[148:151]\n"
    "s_waitcnt lgkmcnt(4)\n"
    "v_mfma_f32_16x16x32_bf16 v[168:171], v[8:11], v[180:183], v[168:171]\n"
    "ds_read_b128 v[156:159], v1 offset:2880\n"
    "v_mfma_f32_16x16x32_bf16 v[192:195], v[12:15], v[180:183], v[192:195]\n"
    "ds_read_b128 v[204:207], v1 offset:2304\n"
    "s_waitcnt lgkmcnt(4)\n"
    "v_mfma_f32_16x16x32_bf16 v[192:195], v[8:11], v[212:215], v[192:195]\n"
    "v_mfma_f32_16x16x32_bf16 v[168:171], v[24:27], v[220:223], v[168:171]\n"
    "ds_read_b128 v[152:155], v1 offset:2816\n"
    "v_mfma_f32_16x16x32_bf16 v[192:195], v[28:31], v[220:223], v[192:195]\n"
    "ds_read_b128 v[216:219], v1 offset:2240\n"
    "s_waitcnt lgkmcnt(4)\n"
    "v_mfma_f32_16x16x32_bf16 v[192:195], v[24:27], v[188:191], v[192:195]\n"
    "v_mfma_f32_16x16x32_bf16 v[168:171], v[40:43], v[160:163], v[168:171]\n"
    "ds_read_b128 v[224:227], v1 offset:2752\n"
    "v_mfma_f32_16x16x32_bf16 v[192:195], v[44:47], v[160:163], v[192:195]\n"
    "ds_read_b128 v[200:203], v1 offset:2176\n"
    "s_waitcnt lgkmcnt(4)\n"
    "v_mfma_f32_16x16x32_bf16 v[192:195], v[40:43], v[156:159], v[192:195]\n"
    "v_mfma_f32_16x16x32_bf16 v[168:171], v[56:59], v[204:207], v[168:171]\n"
    "ds_read_b128 v[180:183], v1 offset:2688\n"
    "v_mfma_f32_16x16x32_bf16 v[192:195], v[60:63], v[204:207], v[192:195]\n"
    "ds_read_b128 v[212:215], v1 offset:2112\n"
    "s_waitcnt lgkmcnt(4)\n"
    "v_mfma_f32_16x16x32_bf16 v[192:195], v[56:59], v[152:155], v[192:195]\n"
    "v_mfma_f32_16x16x32_bf16 v[168:171], v[72:75], v[216:219], v[168:171]\n"
    "ds_read_b128 v[220:223], v1 offset:2624\n"
    "v_mfma_f32_16x16x32_bf16 v[192:195], v[76:79], v[216:219], v[192:195]\n"
    "ds_read_b128 v[188:191], v1 offset:2048\n"
    "s_waitcnt lgkmcnt(4)\n"
    "v_mfma_f32_16x16x32_bf16 v[192:195], v[72:75], v[224:227], v[192:195]\n"
    "v_mfma_f32_16x16x32_bf16 v[168:171], v[88:91], v[200:203], v[168:171]\n"
    "ds_read_b128 v[160:163], v1 offset:2560\n"
    "v_mfma_f32_16x16x32_bf16 v[192:195], v[92:95], v[200:203], v[192:195]\n"
    "ds_read_b128 v[156:159], v1 offset:1984\n"
    "s_waitcnt lgkmcnt(4)\n"
    "v_mfma_f32_16x16x32_bf16 v[192:195], v[88:91], v[180:183], v[192:195]\n"
    "ds_read_b128 v[204:207], v1 offset:2496\n"
    "v_mfma_f32_16x16x32_bf16 v[168:171], v[104:107], v[212:215], v[168:171]\n"
    "ds_read_b128 v[152:155], v1 offset:3008\n"
    "v_mfma_f32_16x16x32_bf16 v[192:195], v[108:111], v[212:215], v[192:195]\n"
    "ds_read_b128 v[216:219], v1 offset:3520\n"
    "s_waitcnt lgkmcnt(5)\n"
    "v_mfma_f32_16x16x32_bf16 v[192:195], v[104:107], v[220:223], v[192:195]\n"
    "v_mfma_f32_16x16x32_bf16 v[168:171], v[120:123], v[188:191], v[168:171]\n"
    "v_mfma_f32_16x16x32_bf16 v[192:195], v[124:127], v[188:191], v[192:195]\n"
    "s_waitcnt lgkmcnt(3)\n"
    "v_mfma_f32_16x16x32_bf16 v[192:195], v[120:123], v[160:163], v[192:195]\n"
    "v_mfma_f32_16x16x32_bf16 v[184:187], v[20:23], v[156:159], v[184:187]\n"
    "ds_read_b128 v[224:227], v1 offset:4032\n"
    "s_waitcnt lgkmcnt(2)\n"
    "v_mfma_f32_16x16x32_bf16 v[184:187], v[16:19], v[204:207], v[184:187]\n"
    "ds_read_b128 v[200:203], v1 offset:1920\n"
    "v_mfma_f32_16x16x32_bf16 v[184:187], v[12:15], v[152:155], v[184:187]\n"
    "ds_read_b128 v[180:183], v1 offset:2432\n"
    "s_waitcnt lgkmcnt(3)\n"
    "v_mfma_f32_16x16x32_bf16 v[184:187], v[8:11], v[216:219], v[184:187]\n"
    "ds_read_b128 v[212:215], v1 offset:2944\n"
    "v_mfma_f32_16x16x32_bf16 v[148:151], v[144:147], v[156:159], v[148:151]\n"
    "ds_read_b128 v[220:223], v1 offset:3456\n"
    "ds_write_b128 v2, v[168:171] offset:5120\n"
    "v_mfma_f32_16x16x32_bf16 v[148:151], v[20:23], v[204:207], v[148:151]\n"
    "ds_write_b128 v2, v[192:195] offset:6400\n"
    "ds_write_b8 v6, v5 offset:768\n"
    "v_mfma_f32_16x16x32_bf16 v[148:151], v[16:19], v[152:155], v[148:151]\n"
    "v_mfma_f32_16x16x32_bf16 v[148:151], v[12:15], v[216:219], v[148:151]\n"
    "s_waitcnt lgkmcnt(6)\n"
    "v_mfma_f32_16x16x32_bf16 v[148:151], v[8:11], v[224:227], v[148:151]\n"
    "v_mfma_f32_16x16x32_bf16 v[184:187], v[36:39], v[200:203], v[184:187]\n"
    "ds_read_b128 v[188:191], v1 offset:3968\n"
    "s_waitcnt lgkmcnt(5)\n"
    "v_mfma_f32_16x16x32_bf16 v[184:187], v[32:35], v[180:183], v[184:187]\n"
    "ds_read_b128 v[160:163], v1 offset:1856\n"
    "v_mfma_f32_16x16x32_bf16 v[184:187], v[28:31], v[212:215], v[184:187]\n"
    "ds_read_b128 v[144:147], v1 offset:2368\n"
    "s_waitcnt lgkmcnt(6)\n"
    "v_mfma_f32_16x16x32_bf16 v[184:187], v[24:27], v[220:223], v[184:187]\n"
    "ds_read_b128 v[156:159], v1 offset:2880\n"
    "v_mfma_f32_16x16x32_bf16 v[148:151], v[140:143], v[200:203], v[148:151]\n"
    "ds_read_b128 v[168:171], v1 offset:3392\n"
    "v_mfma_f32_16x16x32_bf16 v[148:151], v[36:39], v[180:183], v[148:151]\n"
    "v_mfma_f32_16x16x32_bf16 v[148:151], v[32:35], v[212:215], v[148:151]\n"
    "v_mfma_f32_16x16x32_bf16 v[148:151], v[28:31], v[220:223], v[148:151]\n"
    "s_waitcnt lgkmcnt(3)\n"
    "v_mfma_f32_16x16x32_bf16 v[148:151], v[24:27], v[188:191], v[148:151]\n"
    "v_mfma_f32_16x16x32_bf16 v[184:187], v[52:55], v[160:163], v[184:187]\n"
    "ds_read_b128 v[20:23], v1 offset:3904\n"
    "s_waitcnt lgkmcnt(2)\n"
    "v_mfma_f32_16x16x32_bf16 v[184:187], v[48:51], v[144:147], v[184:187]\n"
    "ds_read_b128 v[204:207], v1 offset:1792\n"
    "v_mfma_f32_16x16x32_bf16 v[184:187], v[44:47], v[156:159], v[184:187]\n"
    "ds_read_b128 v[192:195], v1 offset:2304\n"
    "s_waitcnt lgkmcnt(3)\n"
    "v_mfma_f32_16x16x32_bf16 v[184:187], v[40:43], v[168:171], v[184:187]\n"
    "ds_read_b128 v[16:19], v1 offset:2816\n"
    "v_mfma_f32_16x16x32_bf16 v[148:151], v[172:175], v[160:163], v[148:151]\n"
    "ds_read_b128 v[152:155], v1 offset:3328\n"
    "v_mfma_f32_16x16x32_bf16 v[148:151], v[52:55], v[144:147], v[148:151]\n"
    "v_mfma_f32_16x16x32_bf16 v[148:151], v[48:51], v[156:159], v[148:151]\n"
    "v_mfma_f32_16x16x32_bf16 v[148:151], v[44:47], v[168:171], v[148:151]\n"
    "s_waitcnt lgkmcnt(3)\n"
    "v_mfma_f32_16x16x32_bf16 v[148:151], v[40:43], v[20:23], v[148:151]\n"
    "v_mfma_f32_16x16x32_bf16 v[184:187], v[68:71], v[204:207], v[184:187]\n"
    "ds_read_b128 v[12:15], v1 offset:3840\n"
    "s_waitcnt lgkmcnt(2)\n"
    "v_mfma_f32_16x16x32_bf16 v[184:187], v[64:67], v[192:195], v[184:187]\n"
    "ds_read_b128 v[216:219], v1 offset:1728\n"
    "v_mfma_f32_16x16x32_bf16 v[184:187], v[60:63], v[16:19], v[184:187]\n"
    "ds_read_b128 v[8:11], v1 offset:2240\n"
    "s_waitcnt lgkmcnt(3)\n"
    "v_mfma_f32_16x16x32_bf16 v[184:187], v[56:59], v[152:155], v[184:187]\n"
    "ds_read_b128 v[224:227], v1 offset:2752\n"
    "v_mfma_f32_16x16x32_bf16 v[148:151], v[164:167], v[204:207], v[148:151]\n"
    "ds_read_b128 v[140:143], v1 offset:3264\n"
    "v_mfma_f32_16x16x32_bf16 v[148:151], v[68:71], v[192:195], v[148:151]\n"
    "v_mfma_f32_16x16x32_bf16 v[148:151], v[64:67], v[16:19], v[148:151]\n"
    "v_mfma_f32_16x16x32_bf16 v[148:151], v[60:63], v[152:155], v[148:151]\n"
    "s_waitcnt lgkmcnt(3)\n"
    "v_mfma_f32_16x16x32_bf16 v[148:151], v[56:59], v[12:15], v[148:151]\n"
    "v_mfma_f32_16x16x32_bf16 v[184:187], v[84:87], v[216:219], v[184:187]\n"
    "ds_read_b128 v[200:203], v1 offset:3776\n"
    "s_waitcnt lgkmcnt(2)\n"
    "v_mfma_f32_16x16x32_bf16 v[184:187], v[80:83], v[8:11], v[184:187]\n"
    "ds_read_b128 v[36:39], v1 offset:1664\n"
    "v_mfma_f32_16x16x32_bf16 v[184:187], v[76:79], v[224:227], v[184:187]\n"
    "ds_read_b128 v[180:183], v1 offset:2176\n"
    "s_waitcnt lgkmcnt(3)\n"
    "v_mfma_f32_16x16x32_bf16 v[184:187], v[72:75], v[140:143], v[184:187]\n"
    "ds_read_b128 v[32:35], v1 offset:2688\n"
    "v_mfma_f32_16x16x32_bf16 v[148:151], v[136:139], v[216:219], v[148:151]\n"
    "ds_read_b128 v[212:215], v1 offset:3200\n"
    "v_mfma_f32_16x16x32_bf16 v[148:151], v[84:87], v[8:11], v[148:151]\n"
    "v_mfma_f32_16x16x32_bf16 v[148:151], v[80:83], v[224:227], v[148:151]\n"
    "v_mfma_f32_16x16x32_bf16 v[148:151], v[76:79], v[140:143], v[148:151]\n"
    "s_waitcnt lgkmcnt(3)\n"
    "v_mfma_f32_16x16x32_bf16 v[148:151], v[72:75], v[200:203], v[148:151]\n"
    "v_mfma_f32_16x16x32_bf16 v[184:187], v[100:103], v[36:39], v[184:187]\n"
    "ds_read_b128 v[28:31], v1 offset:3712\n"
    "s_waitcnt lgkmcnt(2)\n"
    "v_mfma_f32_16x16x32_bf16 v[184:187], v[96:99], v[180:183], v[184:187]\n"
    "ds_read_b128 v[220:223], v1 offset:1600\n"
    "v_mfma_f32_16x16x32_bf16 v[184:187], v[92:95], v[32:35], v[184:187]\n"
    "ds_read_b128 v[24:27], v1 offset:2112\n"
    "s_waitcnt lgkmcnt(3)\n"
    "v_mfma_f32_16x16x32_bf16 v[184:187], v[88:91], v[212:215], v[184:187]\n"
    "ds_read_b128 v[188:191], v1 offset:2624\n"
    "v_mfma_f32_16x16x32_bf16 v[148:151], v[176:179], v[36:39], v[148:151]\n"
    "ds_read_b128 v[172:175], v1 offset:3136\n"
    "v_mfma_f32_16x16x32_bf16 v[148:151], v[100:103], v[180:183], v[148:151]\n"
    "v_mfma_f32_16x16x32_bf16 v[148:151], v[96:99], v[32:35], v[148:151]\n"
    "v_mfma_f32_16x16x32_bf16 v[148:151], v[92:95], v[212:215], v[148:151]\n"
    "s_waitcnt lgkmcnt(3)\n"
    "v_mfma_f32_16x16x32_bf16 v[148:151], v[88:91], v[28:31], v[148:151]\n"
    "v_mfma_f32_16x16x32_bf16 v[184:187], v[116:119], v[220:223], v[184:187]\n"
    "ds_read_b128 v[160:163], v1 offset:3648\n"
    "s_waitcnt lgkmcnt(2)\n"
    "v_mfma_f32_16x16x32_bf16 v[184:187], v[112:115], v[24:27], v[184:187]\n"
    "ds_read_b128 v[52:55], v1 offset:1536\n"
    "v_mfma_f32_16x16x32_bf16 v[184:187], v[108:111], v[188:191], v[184:187]\n"
    "ds_read_b128 v[144:147], v1 offset:2048\n"
    "s_waitcnt lgkmcnt(3)\n"
    "v_mfma_f32_16x16x32_bf16 v[184:187], v[104:107], v[172:175], v[184:187]\n"
    "ds_read_b128 v[48:51], v1 offset:2560\n"
    "v_mfma_f32_16x16x32_bf16 v[148:151], v[208:211], v[220:223], v[148:151]\n"
    "ds_read_b128 v[156:159], v1 offset:3072\n"
    "v_mfma_f32_16x16x32_bf16 v[148:151], v[116:119], v[24:27], v[148:151]\n"
    "v_mfma_f32_16x16x32_bf16 v[148:151], v[112:115], v[188:191], v[148:151]\n"
    "v_mfma_f32_16x16x32_bf16 v[148:151], v[108:111], v[172:175], v[148:151]\n"
    "s_waitcnt lgkmcnt(3)\n"
    "v_mfma_f32_16x16x32_bf16 v[148:151], v[104:107], v[160:163], v[148:151]\n"
    "v_mfma_f32_16x16x32_bf16 v[184:187], v[132:135], v[52:55], v[184:187]\n"
    "ds_read_b128 v[44:47], v1 offset:3584\n"
    "s_waitcnt lgkmcnt(2)\n"
    "v_mfma_f32_16x16x32_bf16 v[184:187], v[128:131], v[144:147], v[184:187]\n"
    "v_mfma_f32_16x16x32_bf16 v[184:187], v[124:127], v[48:51], v[184:187]\n"
    "s_waitcnt lgkmcnt(1)\n"
    "v_mfma_f32_16x16x32_bf16 v[184:187], v[120:123], v[156:159], v[184:187]\n"
    "v_mfma_f32_16x16x32_bf16 v[148:151], v[196:199], v[52:55], v[148:151]\n"
    "v_mfma_f32_16x16x32_bf16 v[148:151], v[132:135], v[144:147], v[148:151]\n"
    "v_mfma_f32_16x16x32_bf16 v[148:151], v[128:131], v[48:51], v[148:151]\n"
    "v_mfma_f32_16x16x32_bf16 v[148:151], v[124:127], v[156:159], v[148:151]\n"
    "s_waitcnt lgkmcnt(0)\n"
    "v_mfma_f32_16x16x32_bf16 v[148:151], v[120:123], v[44:47], v[148:151]\n"
    "s_nop 1\n"
    "ds_write_b128 v2, v[184:187] offset:7680\n"
    "s_nop 4\n"
    "ds_write_b128 v2, v[148:151] offset:8960\n"
    "ds_write_b8 v6, v5 offset:1024\n"
    "s_branch Lendc\n"
    "Lh2c:\n"
    "s_mov_b32 s4, 4194304\n"
    "Lh2sc:\n"
    "ds_read_b32 v4, v3 offset:256\n"
    "s_waitcnt lgkmcnt(0)\n"
    "v_readfirstlane_b32 s5, v4\n"
    "s_sub_u32 s4, s4, 1\n"
    "s_cbranch_scc1 Lh2scx\n"
    "s_cmp_lg_u32 s5, 0x01010101\n"
    "s_cbranch_scc1 Lh2sc\n"
    "Lh2scx:\n"
    "s_branch Lh2rc\n"
    "Lendc:\n"
    "s_endpgm\n"
    "Lend:\n"
        : "+{v0}"(tid) : "{s[0:1]}"(__builtin_amdgcn_kernarg_segment_ptr()), "{s2}"(__builtin_amdgcn_workgroup_id_x())
        : "memory", "vcc", "scc", "v1", "v2", "v3", "v4", "v5", "v6", "v7", "v8", "v9", "v10", "v11", "v12", "v13", "v14", "v15", "v16", "v17", "v18", "v19", "v20", "v21", "v22", "v23", "v24", "v25", "v26", "v27", "v28", "v29", "v30", "v31", "v32", "v33", "v34", "v35", "v36", "v37", "v38", "v39", "v40", "v41", "v42", "v43", "v44", "v45", "v46", "v47", "v48", "v49", "v50", "v51", "v52", "v53", "v54", "v55", "v56", "v57", "v58", "v59", "v60", "v61", "v62", "v63", "v64", "v65", "v66", "v67", "v68", "v69", "v70", "v71", "v72", "v73", "v74", "v75", "v76", "v77", "v78", "v79", "v80", "v81", "v82", "v83", "v84", "v85", "v86", "v87", "v88", "v89", "v90", "v91", "v92", "v93", "v94", "v95", "v96", "v97", "v98", "v99", "v100", "v101", "v102", "v103", "v104", "v105", "v106", "v107", "v108", "v109", "v110", "v111", "v112", "v113", "v114", "v115", "v116", "v117", "v118", "v119", "v120", "v121", "v122", "v123", "v124", "v125", "v126", "v127", "v128", "v129", "v130", "v131", "v132", "v133", "v134", "v135", "v136", "v137", "v138", "v139", "v140", "v141", "v142", "v143", "v144", "v145", "v146", "v147", "v148", "v149", "v150", "v151", "v152", "v153", "v154", "v155", "v156", "v157", "v158", "v159", "v160", "v161", "v162", "v163", "v164", "v165", "v166", "v167", "v168", "v169", "v170", "v171", "v172", "v173", "v174", "v175", "v176", "v177", "v178", "v179", "v180", "v181", "v182", "v183", "v184", "v185", "v186", "v187", "v188", "v189", "v190", "v191", "v192", "v193", "v194", "v195", "v196", "v197", "v198", "v199", "v200", "v201", "v202", "v203", "v204", "v205", "v206", "v207", "v208", "v209", "v210", "v211", "v212", "v213", "v214", "v215", "v216", "v217", "v218", "v219", "v220", "v221", "v222", "v223", "v224", "v225", "v226", "v227", "v228", "v229", "v230", "v231", "v232", "v233", "v234", "v235", "v236", "v237", "v238", "v239", "v240", "v241", "v242", "v243", "s3", "s4", "s5", "s6", "s7", "s8", "s9", "s10", "s11", "s12", "s13", "s14", "s15", "s16", "s17", "s18", "s19", "s20", "s21", "s22", "s23", "s24", "s25", "s26", "s27", "s28", "s29", "s30", "s31", "s32", "s33", "s34", "s35", "s36", "s37", "s38", "s39", "s40", "s41", "s42", "s43", "s44", "s45", "s46", "s47", "s48", "s49", "s50", "s51", "s52", "s53", "s54", "s55", "s56", "s57", "s58", "s59", "s60", "s61", "s62", "s63", "s64", "s65", "s66", "s67", "s68", "s69", "s70", "s71", "s72", "s73", "s74", "s75", "s76", "s77", "s78", "s79", "s80", "s81", "s82", "s83", "s84", "s85", "s86", "s87", "s88", "s89", "s90", "s91", "s92", "s93", "s94", "s95", "s96", "s97", "s98", "s99", "s100", "s101");
    asm volatile("" :: "v"((u32)(size_t)(__attribute__((address_space(3))) char*)smem + tid));
#endif
}

extern "C" void kernel_launch(void* const* d_in, const int* in_sizes, int n_in, void* d_out, int out_size, void* d_ws, size_t ws_size, hipStream_t stream) {
    const u32 R = 0x00020000u, SD = S * D * 2, W = 2211840;
    fused<<<256, 512, 0, stream>>>(d_in[1], SD, R, d_in[2], SD, R, d_in[3], D * 8, R, d_ws, 5924864, R,
                                   d_in[4], SD, R, d_in[5], D * 2, R, d_in[0], SD, R, d_out, SD, R,
                                   d_in[7], D * 2, R, d_in[8], W, R, d_in[9], W, R, d_in[10], W, R);
}
```
